```python
import torch

def _softmax(x, axis=-1):
    return torch.softmax(x, dim=axis)
def _gelu(x):
    return torch.nn.functional.gelu(x, approximate="tanh")
def _silu(x):
    return torch.nn.functional.silu(x)
def _sigmoid(x):
    return torch.sigmoid(x)
def _layernorm(x, axis=-1, eps=1e-5):
    m = x.mean(dim=axis, keepdim=True)
    v = x.var(dim=axis, keepdim=True, unbiased=False)
    return (x - m) / torch.sqrt(v + eps)
def _scatter_add(tgt, idx, src, axis=0):
    return tgt.clone().index_add_(axis, idx, src)
def _segment_sum(src, seg, n):
    out = torch.zeros((n,) + src.shape[1:], dtype=src.dtype)
    return out.index_add_(0, seg, src)
def _pad(x, pw):
    flat = tuple(v for pair in reversed(pw) for v in pair)
    return torch.nn.functional.pad(x, flat)

kernel_name = 'standalone__mha_block_cached__near_ridge'

def setup_inputs():

    _w = (lambda a: (lambda t: t.to(torch.bfloat16) if t.is_floating_point() else t)(torch.as_tensor(a)))
    B, Sq, D = 16, 32, 1024
    H, Sk, Dh = 32, 32, 128
    return {
        "x": _w((lambda *s: torch.randn(*s, dtype=torch.bfloat16) * 0.8)(B, Sq, D)),
        "k_cache": _w((lambda *s: torch.randn(*s, dtype=torch.bfloat16) * 0.8)(B, H, Sk, Dh)),
        "v_cache": _w((lambda *s: torch.randn(*s, dtype=torch.bfloat16) * 0.05)(B, H, Sk, Dh)),
        "wq": _w((lambda *s: torch.randn(*s, dtype=torch.bfloat16) * 0.8)(D, H * Dh) * (D ** -0.5)),
        "wo": _w((lambda *s: torch.randn(*s, dtype=torch.bfloat16) * 0.05)(H * Dh, D) * ((H * Dh) ** -0.5)),
    }

def reference(x, k_cache, v_cache, wq, wo):
    B, Sq, D = x.shape
    H, Dh = 32, 128
    q = (lambda x, *d: x.permute(*d))((x @ wq).reshape(B, Sq, H, Dh), 0, 2, 1, 3)
    scores = torch.einsum("bhqd,bhkd->bhqk", q, k_cache) * (Dh ** -0.5)
    p = _softmax(scores, axis=-1)
    o = torch.einsum("bhqk,bhkd->bhqd", p, v_cache)
    return (lambda x, *d: x.permute(*d))(o, 0, 2, 1, 3).reshape(B, Sq, H * Dh) @ wo
```

```cpp
#include <hip/hip_runtime.h>
#include <hip/hip_bf16.h>
#include <stdint.h>

typedef short bf16x8 __attribute__((ext_vector_type(8)));
typedef float f32x4 __attribute__((ext_vector_type(4)));
typedef unsigned u32x4 __attribute__((ext_vector_type(4)));

#define DI __device__ __forceinline__

constexpr int NB = 16, SQ = 32, DM = 1024, NH = 32, SK = 32, DH = 128;
constexpr int MROWS = NB * SQ;
constexpr int HD = NH * DH;

DI f32x4 mfma16(u32x4 a, u32x4 b, f32x4 c) {
  return __builtin_amdgcn_mfma_f32_16x16x32_bf16(__builtin_bit_cast(bf16x8, a), __builtin_bit_cast(bf16x8, b), c, 0, 0, 0);
}
DI unsigned bf16_bits(float f) {
  __hip_bfloat16 h = __float2bfloat16(f);
  return (unsigned)__builtin_bit_cast(unsigned short, h);
}
DI float bf16_round(float f) { return __uint_as_float(bf16_bits(f) << 16); }
DI unsigned pack2(float lo, float hi) { return bf16_bits(lo) | (bf16_bits(hi) << 16); }

DI void transpose8x8(const u32x4 (&raw)[8], u32x4 (&out)[8]) {
#pragma unroll
  for (int d = 0; d < 4; ++d) {
#pragma unroll
    for (int p = 0; p < 4; ++p) {
      out[2 * d][p]     = __builtin_amdgcn_perm(raw[2 * p + 1][d], raw[2 * p][d], 0x05040100u);
      out[2 * d + 1][p] = __builtin_amdgcn_perm(raw[2 * p + 1][d], raw[2 * p][d], 0x07060302u);
    }
  }
}

template <int LDA, int LDB>
DI void load_stage(u32x4 (&sa)[4], u32x4 (&sb)[8], const char* const (&Abase)[4], unsigned a_off, const char* Bbase, unsigned b_off, int step) {
#pragma unroll
  for (int m = 0; m < 4; ++m)
    sa[m] = *(const u32x4*)(Abase[m] + (size_t)(step * 64) + a_off);
#pragma unroll
  for (int i = 0; i < 8; ++i)
    sb[i] = *(const u32x4*)(Bbase + (size_t)((step * 32 + i) * LDB * 2) + b_off);
}

DI void compute_stage(const u32x4 (&sa)[4], const u32x4 (&bt)[8], f32x4 (&acc)[4][8]) {
#pragma unroll
  for (int j = 0; j < 8; ++j)
#pragma unroll
    for (int m = 0; m < 4; ++m) acc[m][j] = mfma16(sa[m], bt[j], acc[m][j]);
}

template <int LDA, int LDB, int NSTEP>
DI void gemm_wave(const char* A0, unsigned a_off, const char* Bbase, unsigned b_off, f32x4 (&acc)[4][8], int wave) {
  constexpr int DEPTH = 3;
  const char* Abase[4];
#pragma unroll
  for (int m = 0; m < 4; ++m) Abase[m] = A0 + (size_t)(((wave + m) & 3) * 16 * LDA * 2);
  u32x4 sa[DEPTH][4], sb[DEPTH][8];
#pragma unroll
  for (int s = 0; s < DEPTH; ++s) load_stage<LDA, LDB>(sa[s], sb[s], Abase, a_off, Bbase, b_off, s);
  __builtin_amdgcn_sched_barrier(0);
#pragma unroll
  for (int s = 0; s < NSTEP; ++s) {
    u32x4 bt[8], at[4];
    transpose8x8(sb[s % DEPTH], bt);
#pragma unroll
    for (int m = 0; m < 4; ++m) at[m] = sa[s % DEPTH][m];
    __builtin_amdgcn_sched_barrier(0);
    if (s + DEPTH < NSTEP) load_stage<LDA, LDB>(sa[s % DEPTH], sb[s % DEPTH], Abase, a_off, Bbase, b_off, s + DEPTH);
    __builtin_amdgcn_sched_barrier(0);
    compute_stage(at, bt, acc);
    __builtin_amdgcn_sched_barrier(0);
  }
}

constexpr int RED_BYTES = 4 * 3 * 8 * 1024;
DI void reduce_waves(char* lds, const f32x4 (&acc)[4][8], f32x4 (&res)[8], int wave, int lane) {
#pragma unroll
  for (int m = 1; m < 4; ++m)
#pragma unroll
    for (int j = 0; j < 8; ++j) *(f32x4*)(lds + ((wave * 3 + (m - 1)) * 8 + j) * 1024 + lane * 16) = acc[m][j];
  __syncthreads();
#pragma unroll
  for (int j = 0; j < 8; ++j) res[j] = acc[0][j];
#pragma unroll
  for (int m = 1; m < 4; ++m) {
    const int w = (wave - m) & 3;
#pragma unroll
    for (int j = 0; j < 8; ++j) res[j] += *(const f32x4*)(lds + ((w * 3 + (m - 1)) * 8 + j) * 1024 + lane * 16);
  }
}

constexpr int QROW = 272;
constexpr int K1_LDS = RED_BYTES + 4 * 16 * QROW;

__global__ __launch_bounds__(256, 1) void k_qproj_attn(const __hip_bfloat16* __restrict__ x,
                                                       const __hip_bfloat16* __restrict__ kc,
                                                       const __hip_bfloat16* __restrict__ vc,
                                                       const __hip_bfloat16* __restrict__ wq,
                                                       __hip_bfloat16* __restrict__ o) {
  extern __shared__ __attribute__((aligned(16))) char lds[];
  const int tid = threadIdx.x, lane = tid & 63;
  const int wave = __builtin_amdgcn_readfirstlane(tid >> 6);
  const int c = lane & 15, g = lane >> 4;
  const int bid = blockIdx.x;
  const int xg = bid & 7, idx = bid >> 3;
  const int h = xg * 4 + (idx & 3), mt = idx >> 2;

  const int b = mt * 2 + (wave >> 1);
  const char* kbase = (const char*)kc + (size_t)(b * NH + h) * SK * DH * 2;
  const char* vbase = (const char*)vc + (size_t)(b * NH + h) * SK * DH * 2;
  u32x4 kf[2][4], vraw[8];
#pragma unroll
  for (int kt = 0; kt < 2; ++kt)
#pragma unroll
    for (int s = 0; s < 4; ++s) kf[kt][s] = *(const u32x4*)(kbase + (kt * 16 + c) * 256 + s * 64 + g * 16);
#pragma unroll
  for (int e = 0; e < 8; ++e) vraw[e] = *(const u32x4*)(vbase + (16 * (e >> 2) + 4 * g + (e & 3)) * 256 + c * 16);

  f32x4 acc[4][8];
#pragma unroll
  for (int m = 0; m < 4; ++m)
#pragma unroll
    for (int j = 0; j < 8; ++j) acc[m][j] = (f32x4){0.f, 0.f, 0.f, 0.f};

  const char* Abase = (const char*)x + (size_t)(mt * 64) * DM * 2 + wave * 256 * 2;
  const unsigned a_off = (unsigned)(c * DM * 2 + g * 16);
  const char* Bbase = (const char*)wq + (size_t)(wave * 256) * HD * 2 + h * DH * 2;
  const unsigned b_off = (unsigned)(g * 8 * HD * 2 + c * 16);
  gemm_wave<DM, HD, 8>(Abase, a_off, Bbase, b_off, acc, wave);

  f32x4 q[8];
  reduce_waves(lds, acc, q, wave, lane);

  char* qimg = lds + RED_BYTES + wave * 16 * QROW;
#pragma unroll
  for (int r = 0; r < 4; ++r) {
    u32x4 v;
#pragma unroll
    for (int p = 0; p < 4; ++p) v[p] = pack2(q[2 * p][r], q[2 * p + 1][r]);
    *(u32x4*)(qimg + (4 * g + r) * QROW + c * 16) = v;
  }
  __builtin_amdgcn_wave_barrier();
  u32x4 qf[4];
#pragma unroll
  for (int s = 0; s < 4; ++s) qf[s] = *(const u32x4*)(qimg + c * QROW + s * 64 + g * 16);

  f32x4 sacc[2];
#pragma unroll
  for (int kt = 0; kt < 2; ++kt) {
    sacc[kt] = (f32x4){0.f, 0.f, 0.f, 0.f};
#pragma unroll
    for (int s = 0; s < 4; ++s) sacc[kt] = mfma16(kf[kt][s], qf[s], sacc[kt]);
  }
  const float scale = 0.08838834764831845f;
  float sv[8];
#pragma unroll
  for (int e = 0; e < 8; ++e) sv[e] = bf16_round(bf16_round(sacc[e >> 2][e & 3]) * scale);
  float mx = sv[0];
#pragma unroll
  for (int e = 1; e < 8; ++e) mx = fmaxf(mx, sv[e]);
  mx = fmaxf(mx, __shfl_xor(mx, 16));
  mx = fmaxf(mx, __shfl_xor(mx, 32));
  float sum = 0.f;
#pragma unroll
  for (int e = 0; e < 8; ++e) { sv[e] = __expf(sv[e] - mx); sum += sv[e]; }
  sum += __shfl_xor(sum, 16);
  sum += __shfl_xor(sum, 32);
  const float inv = 1.0f / sum;
  u32x4 pf;
#pragma unroll
  for (int p = 0; p < 4; ++p) pf[p] = pack2(sv[2 * p] * inv, sv[2 * p + 1] * inv);

  u32x4 vt[8];
  transpose8x8(vraw, vt);
  f32x4 oacc[8];
#pragma unroll
  for (int j = 0; j < 8; ++j) oacc[j] = mfma16(pf, vt[j], (f32x4){0.f, 0.f, 0.f, 0.f});

  char* obase = (char*)o + (size_t)(mt * 64 + wave * 16) * HD * 2 + h * DH * 2;
#pragma unroll
  for (int r = 0; r < 4; ++r) {
    u32x4 v;
#pragma unroll
    for (int p = 0; p < 4; ++p) v[p] = pack2(oacc[2 * p][r], oacc[2 * p + 1][r]);
    *(u32x4*)(obase + (size_t)(4 * g + r) * HD * 2 + c * 16) = v;
  }
}

__global__ __launch_bounds__(256, 1) void k_oproj(const __hip_bfloat16* __restrict__ o,
                                                  const __hip_bfloat16* __restrict__ wo,
                                                  float* __restrict__ slab) {
  extern __shared__ __attribute__((aligned(16))) char lds[];
  const int tid = threadIdx.x, lane = tid & 63;
  const int wave = __builtin_amdgcn_readfirstlane(tid >> 6);
  const int c = lane & 15, g = lane >> 4;
  const int bid = blockIdx.x;
  const int xg = bid & 7, idx = bid >> 3;
  const int ks = xg >> 1, nt = (xg & 1) * 4 + (idx & 3), mt = idx >> 2;

  f32x4 acc[4][8];
#pragma unroll
  for (int m = 0; m < 4; ++m)
#pragma unroll
    for (int j = 0; j < 8; ++j) acc[m][j] = (f32x4){0.f, 0.f, 0.f, 0.f};

  const int k0 = ks * 1024 + wave * 256;
  const char* Abase = (const char*)o + (size_t)(mt * 64) * HD * 2 + k0 * 2;
  const unsigned a_off = (unsigned)(c * HD * 2 + g * 16);
  const char* Bbase = (const char*)wo + (size_t)k0 * DM * 2 + nt * 128 * 2;
  const unsigned b_off = (unsigned)(g * 8 * DM * 2 + c * 16);
  gemm_wave<HD, DM, 8>(Abase, a_off, Bbase, b_off, acc, wave);

  f32x4 r[8];
  reduce_waves(lds, acc, r, wave, lane);

  float* sbase = slab + (size_t)ks * MROWS * DM + (size_t)(mt * 64 + wave * 16) * DM + nt * 128;
#pragma unroll
  for (int rr = 0; rr < 4; ++rr) {
    f32x4 v0 = {r[0][rr], r[1][rr], r[2][rr], r[3][rr]};
    f32x4 v1 = {r[4][rr], r[5][rr], r[6][rr], r[7][rr]};
    float* p = sbase + (size_t)(4 * g + rr) * DM + c * 8;
    *(f32x4*)p = v0;
    *(f32x4*)(p + 4) = v1;
  }
}

__global__ __launch_bounds__(256) void k_reduce(const float* __restrict__ slab, __hip_bfloat16* __restrict__ out) {
  const int i = blockIdx.x * 256 + threadIdx.x;
  const float* p = slab + (size_t)i * 8;
  f32x4 a0 = *(const f32x4*)p, a1 = *(const f32x4*)(p + 4);
#pragma unroll
  for (int s = 1; s < 4; ++s) {
    a0 += *(const f32x4*)(p + (size_t)s * MROWS * DM);
    a1 += *(const f32x4*)(p + (size_t)s * MROWS * DM + 4);
  }
  u32x4 v = {pack2(a0[0], a0[1]), pack2(a0[2], a0[3]), pack2(a1[0], a1[1]), pack2(a1[2], a1[3])};
  *(u32x4*)((char*)out + (size_t)i * 16) = v;
}

extern "C" void kernel_launch(void* const* d_in, const int* in_sizes, int n_in,
                              void* d_out, int out_size, void* d_ws, size_t ws_size,
                              hipStream_t stream) {
  const __hip_bfloat16* x  = (const __hip_bfloat16*)d_in[0];
  const __hip_bfloat16* kc = (const __hip_bfloat16*)d_in[1];
  const __hip_bfloat16* vc = (const __hip_bfloat16*)d_in[2];
  const __hip_bfloat16* wq = (const __hip_bfloat16*)d_in[3];
  const __hip_bfloat16* wo = (const __hip_bfloat16*)d_in[4];
  __hip_bfloat16* out = (__hip_bfloat16*)d_out;
  __hip_bfloat16* o = (__hip_bfloat16*)d_ws;
  float* slab = (float*)((char*)d_ws + (size_t)MROWS * HD * 2);

  hipFuncSetAttribute((const void*)k_qproj_attn, hipFuncAttributeMaxDynamicSharedMemorySize, K1_LDS);
  hipFuncSetAttribute((const void*)k_oproj, hipFuncAttributeMaxDynamicSharedMemorySize, RED_BYTES);
  k_qproj_attn<<<256, 256, K1_LDS, stream>>>(x, kc, vc, wq, o);
  k_oproj<<<256, 256, RED_BYTES, stream>>>(o, wo, slab);
  k_reduce<<<MROWS * DM / 8 / 256, 256, 0, stream>>>(slab, out);
}
```

```cpp
#include <hip/hip_runtime.h>
#include <hip/hip_bf16.h>
#include <stdint.h>

typedef short bf16x8 __attribute__((ext_vector_type(8)));
typedef float f32x4 __attribute__((ext_vector_type(4)));
typedef unsigned u32x4 __attribute__((ext_vector_type(4)));
typedef unsigned long long u64;

#define DI __device__ __forceinline__
#define STAMP(K,i)
#define RSTAMP(K,i)
#define DRAIN_STAMP()

constexpr int NB = 16, SQ = 32, DM = 1024, NH = 32, SK = 32, DH = 128;
constexpr int MROWS = NB * SQ;
constexpr int HD = NH * DH;

constexpr size_t WS_FLAG_O = 0;
constexpr size_t WS_FLAG_S = 256 * 128;
constexpr size_t WS_O = 2 * 256 * 128;
constexpr size_t O_BYTES = (size_t)MROWS * HD * 2;
constexpr size_t WS_SLAB = WS_O + O_BYTES;
constexpr size_t SLAB_BYTES = (size_t)64 * 4 * 32768;
constexpr u64 TAG = 0x9E3779B97F4A7C15ull;
constexpr unsigned SPIN_CAP = 1u << 20;

DI f32x4 mfma16(u32x4 a, u32x4 b, f32x4 c) {
  return __builtin_amdgcn_mfma_f32_16x16x32_bf16(__builtin_bit_cast(bf16x8, a), __builtin_bit_cast(bf16x8, b), c, 0, 0, 0);
}
typedef __bf16 bf16x2_t __attribute__((ext_vector_type(2)));
typedef float f32x2 __attribute__((ext_vector_type(2)));
DI unsigned pack2(float lo, float hi) {
  f32x2 f = {lo, hi};
  return __builtin_bit_cast(unsigned, __builtin_convertvector(f, bf16x2_t));
}
DI float bf16_round(float f) { return __uint_as_float(pack2(f, 0.f) << 16); }

DI void transpose8x8(const u32x4 (&raw)[8], u32x4 (&out)[8]) {
#pragma unroll
  for (int d = 0; d < 4; ++d) {
#pragma unroll
    for (int p = 0; p < 4; ++p) {
      out[2 * d][p]     = __builtin_amdgcn_perm(raw[2 * p + 1][d], raw[2 * p][d], 0x05040100u);
      out[2 * d + 1][p] = __builtin_amdgcn_perm(raw[2 * p + 1][d], raw[2 * p][d], 0x07060302u);
    }
  }
}

DI void glds16(const char* sbase, unsigned voff, unsigned lds_dst) {
  unsigned keep;
  asm volatile("s_mov_b32 %0, m0\n\ts_mov_b32 m0, %3\n\ts_nop 0\n\tglobal_load_lds_dwordx4 %1, %2\n\ts_mov_b32 m0, %0"
               : "=&s"(keep) : "v"(voff), "s"(sbase), "s"(lds_dst) : "memory");
}

typedef __amdgpu_buffer_rsrc_t rsrc_t;
DI rsrc_t make_rsrc(const void* p, unsigned bytes) { return __builtin_amdgcn_make_buffer_rsrc((void*)p, 0, (int)bytes, 0x00020000); }
DI u32x4 bload(rsrc_t rs, unsigned voff, unsigned soff) { return __builtin_amdgcn_raw_buffer_load_b128(rs, (int)voff, (int)soff, 0); }
DI u32x4 bload_sc1(rsrc_t rs, unsigned voff, unsigned soff) { return __builtin_amdgcn_raw_buffer_load_b128(rs, (int)voff, (int)soff, 16); }
DI void bstore_sc1(u32x4 v, rsrc_t rs, unsigned voff, unsigned soff) { __builtin_amdgcn_raw_buffer_store_b128(v, rs, (int)voff, (int)soff, 16); }

DI void compute_stage(const u32x4 (&sa)[4], const u32x4 (&bt)[8], f32x4 (&acc)[4][8]) {
#pragma unroll
  for (int j = 0; j < 8; ++j)
#pragma unroll
    for (int m = 0; m < 4; ++m) acc[m][j] = mfma16(sa[m], bt[j], acc[m][j]);
}

DI void reduce_waves(char* lds, const f32x4 (&acc)[4][8], f32x4 (&res)[8], int wave, int lane) {
#pragma unroll
  for (int m = 1; m < 4; ++m)
#pragma unroll
    for (int j = 0; j < 8; ++j) *(f32x4*)(lds + wave * 32768 + ((m - 1) * 8 + j) * 1024 + lane * 16) = acc[m][j];
  __syncthreads();
#pragma unroll
  for (int j = 0; j < 8; ++j) res[j] = acc[0][j];
#pragma unroll
  for (int m = 1; m < 4; ++m) {
    const int w = (wave - m) & 3;
#pragma unroll
    for (int j = 0; j < 8; ++j) res[j] += *(const f32x4*)(lds + w * 32768 + ((m - 1) * 8 + j) * 1024 + lane * 16);
  }
}

#define INTERLEAVE_STEP()                                                                   \
  _Pragma("unroll") for (int i_ = 0; i_ < 32; ++i_) {                                       \
    __builtin_amdgcn_sched_group_barrier(0x008, 1, 0);                                      \
    if (i_ < 24 && (i_ & 1)) __builtin_amdgcn_sched_group_barrier(0x020, 1, 0);             \
    if (i_ >= 8 && i_ < 20) __builtin_amdgcn_sched_group_barrier(0x100, 1, 0);              \
    if (i_ >= 16) __builtin_amdgcn_sched_group_barrier(0x002, 2, 0);                        \
  }

constexpr int QROW = 272;
constexpr int LDS_BYTES = 4 * 32768;

__global__ __launch_bounds__(256, 1) void k_fused(const __hip_bfloat16* __restrict__ x,
                                                  const __hip_bfloat16* __restrict__ kc,
                                                  const __hip_bfloat16* __restrict__ vc,
                                                  const __hip_bfloat16* __restrict__ wq,
                                                  const __hip_bfloat16* __restrict__ wo,
                                                  char* __restrict__ ws,
                                                  __hip_bfloat16* __restrict__ out) {
  extern __shared__ __attribute__((aligned(16))) char lds[];
  const int tid = threadIdx.x, lane = tid & 63;
  const int wave = __builtin_amdgcn_readfirstlane(tid >> 6);
  const int c = lane & 15, g = lane >> 4;
  const int bid = blockIdx.x;
  const int xg = bid & 7, idx = bid >> 3;
  const int mt = idx >> 2;
  const int h = xg + 8 * (idx & 3);
  const int ks = xg >> 1, nt = (xg & 1) * 4 + (idx & 3);
  RSTAMP(0,6); STAMP(0,0);

  char* abuf = lds + wave * 32768;
  const unsigned abuf_l = (unsigned)(uintptr_t)(__attribute__((address_space(3))) char*)abuf;
  f32x4 acc[4][8];
#pragma unroll
  for (int m = 0; m < 4; ++m)
#pragma unroll
    for (int j = 0; j < 8; ++j) acc[m][j] = (f32x4){0.f, 0.f, 0.f, 0.f};

  const char* xrow = (const char*)x + (size_t)(mt * 64) * (DM * 2) + wave * 512;
  auto dma_x = [&](int hh, int i) {
    const int row = 4 * i + g;
    const unsigned off = (unsigned)(row * (DM * 2) + hh * 256 + ((c ^ (row & 15)) << 4));
    glds16(xrow, off, abuf_l + hh * 16384 + i * 1024);
  };
  const rsrc_t rq = make_rsrc((const char*)wq + (size_t)(wave * 256) * HD * 2 + h * DH * 2, 256 * HD * 2);
  const unsigned bq_off = (unsigned)(g * 8 * HD * 2 + c * 16);
  auto load_wq = [&](u32x4 (&sb)[8], int step) {
#pragma unroll
    for (int i = 0; i < 8; ++i) sb[i] = bload(rq, bq_off, (unsigned)((step * 32 + i) * HD * 2));
  };
  const int b = mt * 2 + (wave >> 1);
  const char* kbase = (const char*)kc + (size_t)(b * NH + h) * SK * DH * 2;
  const char* vbase = (const char*)vc + (size_t)(b * NH + h) * SK * DH * 2;
  u32x4 kf[2][4], vraw[8];

  const int k0 = ks * 1024 + wave * 256;
  const char* wo_base = (const char*)wo + (size_t)k0 * DM * 2 + nt * 256;
  const rsrc_t ro = make_rsrc(wo_base, 256 * DM * 2);
  const unsigned bo_off = (unsigned)(g * 32 * DM * 2 + c * 16);
  u32x4 so[4][8];

  {
    constexpr int DEPTH = 3, NSTEP = 8;
    u32x4 sb[DEPTH][8];
#pragma unroll
    for (int i = 0; i < 16; ++i) dma_x(0, i);
    load_wq(sb[0], 0);
    load_wq(sb[1], 1);
    __builtin_amdgcn_sched_barrier(0);
    unsigned aoff[4];
#pragma unroll
    for (int s = 0; s < 4; ++s) aoff[s] = (unsigned)(c * 256 + (((4 * s + g) ^ c) << 4));
    auto read_a = [&](u32x4 (&at)[4], int s) {
#pragma unroll
      for (int m = 0; m < 4; ++m)
        at[m] = *(const u32x4*)(abuf + ((wave + m) & 3) * 4096 + (s >> 2) * 16384 + aoff[s & 3]);
    };
    u32x4 at[2][4], bt[2][8];
    asm volatile("s_waitcnt vmcnt(16)" ::: "memory");
    read_a(at[0], 0);
    transpose8x8(sb[0], bt[0]);
    __builtin_amdgcn_sched_barrier(0);
    STAMP(0,1);
#pragma unroll
    for (int s = 0; s < NSTEP; ++s) {
      if (s < 3) {
#pragma unroll
        for (int i = 0; i < (s == 2 ? 6 : 5); ++i) dma_x(1, s * 5 + i);
      }
      if (s + 2 < NSTEP) load_wq(sb[(s + 2) % DEPTH], s + 2);
      if (s == NSTEP - 2) {
#pragma unroll
        for (int kt = 0; kt < 2; ++kt)
#pragma unroll
          for (int ss = 0; ss < 4; ++ss) kf[kt][ss] = *(const u32x4*)(kbase + (kt * 16 + c) * 256 + ss * 64 + g * 16);
      }
      if (s == NSTEP - 1) {
#pragma unroll
        for (int e = 0; e < 8; ++e) vraw[e] = *(const u32x4*)(vbase + (16 * (e >> 2) + 4 * g + (e & 3)) * 256 + c * 16);
      }
      if (s == 3) asm volatile("s_waitcnt vmcnt(16)" ::: "memory");
      if (s + 1 < NSTEP) { read_a(at[(s + 1) & 1], s + 1); transpose8x8(sb[(s + 1) % DEPTH], bt[(s + 1) & 1]); }
      compute_stage(at[s & 1], bt[s & 1], acc);
      INTERLEAVE_STEP();
      __builtin_amdgcn_sched_barrier(0);
    }
  }
  STAMP(0,2);
#define PF(a, n) do { __builtin_amdgcn_sched_barrier(0); _Pragma("unroll") for (int u_ = (a); u_ < (a) + (n); ++u_) \
    so[u_ >> 3][u_ & 7] = bload(ro, bo_off, (unsigned)((8 * (u_ >> 3) + (u_ & 7)) * DM * 2)); __builtin_amdgcn_sched_barrier(0); } while (0)
  {
    f32x4 q[8];
    PF(0, 3);
#pragma unroll
    for (int m = 1; m < 4; ++m) {
#pragma unroll
      for (int j = 0; j < 8; ++j) *(f32x4*)(lds + wave * 32768 + ((m - 1) * 8 + j) * 1024 + lane * 16) = acc[m][j];
      if (m == 1) PF(3, 2); else if (m == 2) PF(5, 2); else PF(7, 2);
    }
    __syncthreads();
#pragma unroll
    for (int j = 0; j < 8; ++j) q[j] = acc[0][j];
#pragma unroll
    for (int m = 1; m < 4; ++m) {
      const int w = (wave - m) & 3;
#pragma unroll
      for (int j = 0; j < 8; ++j) q[j] += *(const f32x4*)(lds + w * 32768 + ((m - 1) * 8 + j) * 1024 + lane * 16);
      if (m == 1) PF(9, 2); else if (m == 2) PF(11, 2); else PF(13, 2);
    }
    STAMP(0,3);
    char* qimg = abuf + 24 * 1024;
#pragma unroll
    for (int r = 0; r < 4; ++r) {
      u32x4 v;
#pragma unroll
      for (int p = 0; p < 4; ++p) v[p] = pack2(q[2 * p][r], q[2 * p + 1][r]);
      *(u32x4*)(qimg + (4 * g + r) * QROW + c * 16) = v;
    }
    __builtin_amdgcn_wave_barrier();
    PF(15, 3);
    u32x4 qf[4];
#pragma unroll
    for (int s = 0; s < 4; ++s) qf[s] = *(const u32x4*)(qimg + c * QROW + s * 64 + g * 16);

    f32x4 sacc[2];
#pragma unroll
    for (int kt = 0; kt < 2; ++kt) {
      sacc[kt] = (f32x4){0.f, 0.f, 0.f, 0.f};
#pragma unroll
      for (int s = 0; s < 4; ++s) sacc[kt] = mfma16(kf[kt][s], qf[s], sacc[kt]);
    }
    PF(18, 3);
    const float scale = 0.08838834764831845f;
    float sv[8];
#pragma unroll
    for (int e = 0; e < 8; ++e) sv[e] = bf16_round(bf16_round(sacc[e >> 2][e & 3]) * scale);
    float mx = sv[0];
#pragma unroll
    for (int e = 1; e < 8; ++e) mx = fmaxf(mx, sv[e]);
    PF(21, 3);
    mx = fmaxf(mx, __shfl_xor(mx, 16));
    mx = fmaxf(mx, __shfl_xor(mx, 32));
    PF(24, 3);
    float sum = 0.f;
#pragma unroll
    for (int e = 0; e < 8; ++e) { sv[e] = __expf(sv[e] - mx); sum += sv[e]; }
    PF(27, 3);
    sum += __shfl_xor(sum, 16);
    sum += __shfl_xor(sum, 32);
    PF(30, 2);
    const float inv = 1.0f / sum;
    u32x4 pf;
#pragma unroll
    for (int p = 0; p < 4; ++p) pf[p] = pack2(sv[2 * p] * inv, sv[2 * p + 1] * inv);

    u32x4 vt[8];
    transpose8x8(vraw, vt);
    f32x4 oacc[8];
#pragma unroll
    for (int j = 0; j < 8; ++j) oacc[j] = mfma16(vt[j], pf, (f32x4){0.f, 0.f, 0.f, 0.f});

    const rsrc_t rO = make_rsrc(ws + WS_O, (unsigned)O_BYTES);
#pragma unroll
    for (int r = 0; r < 4; ++r) {
      u32x4 v;
#pragma unroll
      for (int p = 0; p < 4; ++p) v[p] = pack2(oacc[2 * p][r], oacc[2 * p + 1][r]);
      bstore_sc1(v, rO, (unsigned)(lane * 16), (unsigned)(((mt * 4 + wave) * 128 + h * 4 + r) * 1024));
    }
  }
  STAMP(0,4);
  asm volatile("s_waitcnt vmcnt(0)" ::: "memory");
  __syncthreads();
  if (wave == 0 && lane < 8) {
    const int cb = (mt * 4 + (lane & 3)) * 8 + (h >> 3) * 2 + (lane >> 2);
    __hip_atomic_store((u64*)(ws + WS_FLAG_O + cb * 128 + (h & 7) * 8), TAG, __ATOMIC_RELAXED, __HIP_MEMORY_SCOPE_AGENT);
  }
  STAMP(0,5);
#pragma unroll
  for (int t = 4; t < 8; ++t)
#pragma unroll
    for (int i = 0; i < 8; ++i)
      glds16(wo_base, bo_off + (unsigned)((128 * (t >> 2) + 8 * (t & 3) + i) * DM * 2), abuf_l + ((t - 4) * 8 + i) * 1024);

  if (wave == 0) {
    u64* fl = (u64*)(ws + WS_FLAG_O + bid * 128) + (lane & 7);
    for (unsigned spin = 0; spin < SPIN_CAP; ++spin) {
      const u64 v = __hip_atomic_load(fl, __ATOMIC_RELAXED, __HIP_MEMORY_SCOPE_AGENT);
      if (__all(v == TAG)) break;
      __builtin_amdgcn_s_sleep(2);
    }
    if (lane < 8) __hip_atomic_store(fl, 0ull, __ATOMIC_RELAXED, __HIP_MEMORY_SCOPE_AGENT);
  }
  __syncthreads();
  __builtin_amdgcn_fence(__ATOMIC_ACQUIRE, "wavefront");
  STAMP(1,0);
#pragma unroll
  for (int m = 0; m < 4; ++m)
#pragma unroll
    for (int j = 0; j < 8; ++j) acc[m][j] = (f32x4){0.f, 0.f, 0.f, 0.f};
  {
    const rsrc_t rA = make_rsrc(ws + WS_O, (unsigned)O_BYTES);
    unsigned a_soff[4];
#pragma unroll
    for (int m = 0; m < 4; ++m) a_soff[m] = (unsigned)(((mt * 4 + ((wave + m) & 3)) * 128 + (k0 >> 5)) * 1024);
    constexpr int DA = 4, NSTEP = 8;
    u32x4 sa[DA][4];
    auto load_a = [&](int d, int t) {
#pragma unroll
      for (int m = 0; m < 4; ++m) sa[d][m] = bload_sc1(rA, (unsigned)(lane * 16), a_soff[m] + t * 1024);
    };
#pragma unroll
    for (int t = 0; t < DA; ++t) load_a(t, t);
    u32x4 bt[2][8];
    transpose8x8(so[0], bt[0]);
    __builtin_amdgcn_sched_barrier(0);
#pragma unroll
    for (int t = 0; t < NSTEP; ++t) {
      u32x4 at[4];
#pragma unroll
      for (int m = 0; m < 4; ++m) at[m] = sa[t % DA][m];
      __builtin_amdgcn_sched_barrier(0);
      if (t == 0) STAMP(1,1);
      if (t + DA < NSTEP) load_a(t % DA, t + DA);
      if (t + 1 < 4) transpose8x8(so[t + 1], bt[(t + 1) & 1]);
      else if (t + 1 < NSTEP) {
        u32x4 raw[8];
#pragma unroll
        for (int i = 0; i < 8; ++i) raw[i] = *(const u32x4*)(abuf + ((t + 1 - 4) * 8 + i) * 1024 + lane * 16);
        transpose8x8(raw, bt[(t + 1) & 1]);
      }
      compute_stage(at, bt[t & 1], acc);
      INTERLEAVE_STEP();
      __builtin_amdgcn_sched_barrier(0);
    }
  }
  STAMP(1,2);
  f32x4 r[8];
  reduce_waves(lds, acc, r, wave, lane);
  STAMP(1,3);

  const int tile = mt * 8 + nt;
  const rsrc_t rs = make_rsrc(ws + WS_SLAB, (unsigned)SLAB_BYTES);
  const unsigned sl_off = (unsigned)((tile * 16 + wave) * 8192 + lane * 16);
#pragma unroll
  for (int j = 0; j < 8; ++j) bstore_sc1(__builtin_bit_cast(u32x4, r[j]), rs, sl_off, (unsigned)(ks * 32768 + j * 1024));
  asm volatile("s_waitcnt vmcnt(0)" ::: "memory");
  __syncthreads();
  if (wave == 0 && lane < 4) {
    const int ob = (mt * 4 + (nt & 3)) * 8 + lane * 2 + (nt >> 2);
    __hip_atomic_store((u64*)(ws + WS_FLAG_S + ob * 128 + ks * 8), TAG, __ATOMIC_RELAXED, __HIP_MEMORY_SCOPE_AGENT);
  }
  STAMP(1,4);
  if (wave != ks) { RSTAMP(1,7); return; }
  {
    u64* fl = (u64*)(ws + WS_FLAG_S + bid * 128) + (lane & 3);
    for (unsigned spin = 0; spin < SPIN_CAP; ++spin) {
      const u64 v = __hip_atomic_load(fl, __ATOMIC_RELAXED, __HIP_MEMORY_SCOPE_AGENT);
      if (__all(v == TAG)) break;
      __builtin_amdgcn_s_sleep(1);
    }
    if (lane < 4) __hip_atomic_store(fl, 0ull, __ATOMIC_RELAXED, __HIP_MEMORY_SCOPE_AGENT);
  }
  __builtin_amdgcn_fence(__ATOMIC_ACQUIRE, "wavefront");
  f32x4 s0[8], s1[8], s2[8], s3[8];
#pragma unroll
  for (int j = 0; j < 8; ++j) {
    s0[j] = __builtin_bit_cast(f32x4, bload_sc1(rs, sl_off, (unsigned)(0 * 32768 + j * 1024)));
    s1[j] = __builtin_bit_cast(f32x4, bload_sc1(rs, sl_off, (unsigned)(1 * 32768 + j * 1024)));
    s2[j] = __builtin_bit_cast(f32x4, bload_sc1(rs, sl_off, (unsigned)(2 * 32768 + j * 1024)));
    s3[j] = __builtin_bit_cast(f32x4, bload_sc1(rs, sl_off, (unsigned)(3 * 32768 + j * 1024)));
  }
#pragma unroll
  for (int j = 0; j < 8; ++j) r[j] = (s0[j] + s1[j]) + (s2[j] + s3[j]);
  char* obase = (char*)out + (size_t)(mt * 64 + wave * 16 + 4 * g) * DM * 2 + nt * 256 + c * 16;
#pragma unroll
  for (int rr = 0; rr < 4; ++rr) {
    u32x4 v;
#pragma unroll
    for (int p = 0; p < 4; ++p) v[p] = pack2(r[2 * p][rr], r[2 * p + 1][rr]);
    *(u32x4*)(obase + (size_t)rr * DM * 2) = v;
  }
  DRAIN_STAMP(); STAMP(1,5); RSTAMP(1,7);
}

extern "C" void kernel_launch(void* const* d_in, const int* in_sizes, int n_in,
                              void* d_out, int out_size, void* d_ws, size_t ws_size,
                              hipStream_t stream) {
  const __hip_bfloat16* x  = (const __hip_bfloat16*)d_in[0];
  const __hip_bfloat16* kc = (const __hip_bfloat16*)d_in[1];
  const __hip_bfloat16* vc = (const __hip_bfloat16*)d_in[2];
  const __hip_bfloat16* wq = (const __hip_bfloat16*)d_in[3];
  const __hip_bfloat16* wo = (const __hip_bfloat16*)d_in[4];
  __hip_bfloat16* out = (__hip_bfloat16*)d_out;
  char* ws = (char*)d_ws;
  (void)hipFuncSetAttribute((const void*)k_fused, hipFuncAttributeMaxDynamicSharedMemorySize, LDS_BYTES);
  k_fused<<<256, 256, LDS_BYTES, stream>>>(x, kc, vc, wq, wo, ws, out);
}
```

```cpp
#include <hip/hip_runtime.h>
#include <hip/hip_bf16.h>
#include <stdint.h>

typedef short bf16x8 __attribute__((ext_vector_type(8)));
typedef float f32x4 __attribute__((ext_vector_type(4)));
typedef unsigned u32x4 __attribute__((ext_vector_type(4)));

#define DI __device__ __forceinline__
#define STAMP(K,i)
#define RSTAMP(K,i)
#define DRAIN_STAMP()

constexpr int NB = 16, SQ = 32, DM = 1024, NH = 32, SK = 32, DH = 128;
constexpr int MROWS = NB * SQ;
constexpr int HD = NH * DH;

constexpr size_t WS_CNT = 0;
constexpr size_t WS_O = 64 * 256;
constexpr size_t WS_SLAB = WS_O + (size_t)MROWS * HD * 2;
constexpr size_t SLAB_BYTES = (size_t)64 * 4 * 32768;

DI f32x4 mfma16(u32x4 a, u32x4 b, f32x4 c) {
  return __builtin_amdgcn_mfma_f32_16x16x32_bf16(__builtin_bit_cast(bf16x8, a), __builtin_bit_cast(bf16x8, b), c, 0, 0, 0);
}
DI unsigned bf16_bits(float f) {
  __hip_bfloat16 h = __float2bfloat16(f);
  return (unsigned)__builtin_bit_cast(unsigned short, h);
}
DI float bf16_round(float f) { return __uint_as_float(bf16_bits(f) << 16); }
DI unsigned pack2(float lo, float hi) { return bf16_bits(lo) | (bf16_bits(hi) << 16); }

DI void transpose8x8(const u32x4 (&raw)[8], u32x4 (&out)[8]) {
#pragma unroll
  for (int d = 0; d < 4; ++d) {
#pragma unroll
    for (int p = 0; p < 4; ++p) {
      out[2 * d][p]     = __builtin_amdgcn_perm(raw[2 * p + 1][d], raw[2 * p][d], 0x05040100u);
      out[2 * d + 1][p] = __builtin_amdgcn_perm(raw[2 * p + 1][d], raw[2 * p][d], 0x07060302u);
    }
  }
}

DI void glds16(const char* sbase, unsigned voff, unsigned lds_dst) {
  unsigned keep;
  asm volatile("s_mov_b32 %0, m0\n\ts_mov_b32 m0, %3\n\ts_nop 0\n\tglobal_load_lds_dwordx4 %1, %2\n\ts_mov_b32 m0, %0"
               : "=&s"(keep) : "v"(voff), "s"(sbase), "s"(lds_dst) : "memory");
}

typedef __amdgpu_buffer_rsrc_t rsrc_t;
DI rsrc_t make_rsrc(const void* p, unsigned bytes) { return __builtin_amdgcn_make_buffer_rsrc((void*)p, 0, (int)bytes, 0x00020000); }
DI u32x4 bload(rsrc_t rs, unsigned voff, unsigned soff) { return __builtin_amdgcn_raw_buffer_load_b128(rs, (int)voff, (int)soff, 0); }

template <int LDB>
DI void load_b(u32x4 (&sb)[8], rsrc_t Bbase, unsigned b_off, int step) {
#pragma unroll
  for (int i = 0; i < 8; ++i) sb[i] = bload(Bbase, b_off, (unsigned)((step * 32 + i) * LDB * 2));
}

DI void compute_stage(const u32x4 (&sa)[4], const u32x4 (&bt)[8], f32x4 (&acc)[4][8]) {
#pragma unroll
  for (int j = 0; j < 8; ++j)
#pragma unroll
    for (int m = 0; m < 4; ++m) acc[m][j] = mfma16(sa[m], bt[j], acc[m][j]);
}

template <int WSTRIDE>
DI void reduce_waves(char* lds, const f32x4 (&acc)[4][8], f32x4 (&res)[8], int wave, int lane) {
#pragma unroll
  for (int m = 1; m < 4; ++m)
#pragma unroll
    for (int j = 0; j < 8; ++j) *(f32x4*)(lds + wave * WSTRIDE + ((m - 1) * 8 + j) * 1024 + lane * 16) = acc[m][j];
  __syncthreads();
#pragma unroll
  for (int j = 0; j < 8; ++j) res[j] = acc[0][j];
#pragma unroll
  for (int m = 1; m < 4; ++m) {
    const int w = (wave - m) & 3;
#pragma unroll
    for (int j = 0; j < 8; ++j) res[j] += *(const f32x4*)(lds + w * WSTRIDE + ((m - 1) * 8 + j) * 1024 + lane * 16);
  }
}

constexpr int QROW = 272;
constexpr int K1_LDS = 4 * 32768;

__global__ __launch_bounds__(256, 1) void k_qproj_attn(const __hip_bfloat16* __restrict__ x,
                                                       const __hip_bfloat16* __restrict__ kc,
                                                       const __hip_bfloat16* __restrict__ vc,
                                                       const __hip_bfloat16* __restrict__ wq,
                                                       char* __restrict__ ws) {
  extern __shared__ __attribute__((aligned(16))) char lds[];
  const int tid = threadIdx.x, lane = tid & 63;
  const int wave = __builtin_amdgcn_readfirstlane(tid >> 6);
  const int c = lane & 15, g = lane >> 4;
  const int bid = blockIdx.x;
  const int xg = bid & 7, idx = bid >> 3;
  const int h = xg + 8 * (idx & 3), mt = idx >> 2;

  if (bid < 64 && tid == 0) *(unsigned*)(ws + WS_CNT + bid * 256) = 0u;
  RSTAMP(0,6); STAMP(0,0);

  char* abuf = lds + wave * 32768;
  const unsigned abuf_l = (unsigned)(uintptr_t)(__attribute__((address_space(3))) char*)abuf;
  const char* xrow = (const char*)x + (size_t)(mt * 64) * (DM * 2) + wave * 512;
  auto dma_piece = [&](int hh, int i) {
    const int row = 4 * i + g;
    const unsigned off = (unsigned)(row * (DM * 2) + hh * 256 + ((c ^ (row & 15)) << 4));
    glds16(xrow, off, abuf_l + hh * 16384 + i * 1024);
  };

  f32x4 acc[4][8];
#pragma unroll
  for (int m = 0; m < 4; ++m)
#pragma unroll
    for (int j = 0; j < 8; ++j) acc[m][j] = (f32x4){0.f, 0.f, 0.f, 0.f};

  const rsrc_t Bbase = make_rsrc((const char*)wq + (size_t)(wave * 256) * HD * 2 + h * DH * 2, 256 * HD * 2);
  const unsigned b_off = (unsigned)(g * 8 * HD * 2 + c * 16);

  const int b = mt * 2 + (wave >> 1);
  const char* kbase = (const char*)kc + (size_t)(b * NH + h) * SK * DH * 2;
  const char* vbase = (const char*)vc + (size_t)(b * NH + h) * SK * DH * 2;
  u32x4 kf[2][4], vraw[8];

  constexpr int DEPTH = 3, NSTEP = 8;
  u32x4 sb[DEPTH][8];
#pragma unroll
  for (int i = 0; i < 16; ++i) dma_piece(0, i);
  load_b<HD>(sb[0], Bbase, b_off, 0);
  load_b<HD>(sb[1], Bbase, b_off, 1);
  __builtin_amdgcn_sched_barrier(0);
  unsigned aoff[4];
#pragma unroll
  for (int s = 0; s < 4; ++s) aoff[s] = (unsigned)(c * 256 + (((4 * s + g) ^ c) << 4));
  auto read_a = [&](u32x4 (&at)[4], int s) {
#pragma unroll
    for (int m = 0; m < 4; ++m)
      at[m] = *(const u32x4*)(abuf + ((wave + m) & 3) * 4096 + (s >> 2) * 16384 + aoff[s & 3]);
  };
  u32x4 at[2][4], bt[2][8];
  asm volatile("s_waitcnt vmcnt(16)" ::: "memory");
  STAMP(0,1);
  read_a(at[0], 0);
  transpose8x8(sb[0], bt[0]);
  __builtin_amdgcn_sched_barrier(0);
  STAMP(0,2);
#pragma unroll
  for (int s = 0; s < NSTEP; ++s) {
    if (s < 3) {
#pragma unroll
      for (int i = 0; i < (s == 2 ? 6 : 5); ++i) dma_piece(1, s * 5 + i);
    }
    if (s + 2 < NSTEP) load_b<HD>(sb[(s + 2) % DEPTH], Bbase, b_off, s + 2);
    if (s == NSTEP - 2) {
#pragma unroll
      for (int kt = 0; kt < 2; ++kt)
#pragma unroll
        for (int ss = 0; ss < 4; ++ss) kf[kt][ss] = *(const u32x4*)(kbase + (kt * 16 + c) * 256 + ss * 64 + g * 16);
    }
    if (s == NSTEP - 1) {
#pragma unroll
      for (int e = 0; e < 8; ++e) vraw[e] = *(const u32x4*)(vbase + (16 * (e >> 2) + 4 * g + (e & 3)) * 256 + c * 16);
    }
    if (s == 3) asm volatile("s_waitcnt vmcnt(16)" ::: "memory");
    if (s + 1 < NSTEP) { read_a(at[(s + 1) & 1], s + 1); transpose8x8(sb[(s + 1) % DEPTH], bt[(s + 1) & 1]); }
    compute_stage(at[s & 1], bt[s & 1], acc);
#pragma unroll
    for (int i = 0; i < 32; ++i) {
      __builtin_amdgcn_sched_group_barrier(0x008, 1, 0);
      if (i < 16 && (i & 1)) __builtin_amdgcn_sched_group_barrier(0x020, 1, 0);
      if (i >= 16 && i < 20) __builtin_amdgcn_sched_group_barrier(0x100, 1, 0);
      if (i >= 16) __builtin_amdgcn_sched_group_barrier(0x002, 2, 0);
    }
    __builtin_amdgcn_sched_barrier(0);
  }

  STAMP(0,3);
  f32x4 q[8];
  reduce_waves<32768>(lds, acc, q, wave, lane);
  STAMP(0,4);

  char* qimg = abuf + 24 * 1024;
#pragma unroll
  for (int r = 0; r < 4; ++r) {
    u32x4 v;
#pragma unroll
    for (int p = 0; p < 4; ++p) v[p] = pack2(q[2 * p][r], q[2 * p + 1][r]);
    *(u32x4*)(qimg + (4 * g + r) * QROW + c * 16) = v;
  }
  __builtin_amdgcn_wave_barrier();
  u32x4 qf[4];
#pragma unroll
  for (int s = 0; s < 4; ++s) qf[s] = *(const u32x4*)(qimg + c * QROW + s * 64 + g * 16);

  f32x4 sacc[2];
#pragma unroll
  for (int kt = 0; kt < 2; ++kt) {
    sacc[kt] = (f32x4){0.f, 0.f, 0.f, 0.f};
#pragma unroll
    for (int s = 0; s < 4; ++s) sacc[kt] = mfma16(kf[kt][s], qf[s], sacc[kt]);
  }
  const float scale = 0.08838834764831845f;
  float sv[8];
#pragma unroll
  for (int e = 0; e < 8; ++e) sv[e] = bf16_round(bf16_round(sacc[e >> 2][e & 3]) * scale);
  float mx = sv[0];
#pragma unroll
  for (int e = 1; e < 8; ++e) mx = fmaxf(mx, sv[e]);
  mx = fmaxf(mx, __shfl_xor(mx, 16));
  mx = fmaxf(mx, __shfl_xor(mx, 32));
  float sum = 0.f;
#pragma unroll
  for (int e = 0; e < 8; ++e) { sv[e] = __expf(sv[e] - mx); sum += sv[e]; }
  sum += __shfl_xor(sum, 16);
  sum += __shfl_xor(sum, 32);
  const float inv = 1.0f / sum;
  u32x4 pf;
#pragma unroll
  for (int p = 0; p < 4; ++p) pf[p] = pack2(sv[2 * p] * inv, sv[2 * p + 1] * inv);

  u32x4 vt[8];
  transpose8x8(vraw, vt);
  f32x4 oacc[8];
#pragma unroll
  for (int j = 0; j < 8; ++j) oacc[j] = mfma16(pf, vt[j], (f32x4){0.f, 0.f, 0.f, 0.f});

  char* obase = ws + WS_O + ((size_t)(mt * 4 + wave) * 128 + h * 4 + (c >> 2)) * 1024 + ((c & 3) * 16 + 4 * g) * 16;
#pragma unroll
  for (int r = 0; r < 4; ++r) {
    u32x4 v;
#pragma unroll
    for (int p = 0; p < 4; ++p) v[p] = pack2(oacc[2 * p][r], oacc[2 * p + 1][r]);
    *(u32x4*)(obase + r * 16) = v;
  }
  DRAIN_STAMP(); STAMP(0,5); RSTAMP(0,7);
}

constexpr int K2_LDS = 4 * 24576 + 16;

__global__ __launch_bounds__(256, 1) void k_oproj(const __hip_bfloat16* __restrict__ wo,
                                                  char* __restrict__ ws,
                                                  __hip_bfloat16* __restrict__ out) {
  extern __shared__ __attribute__((aligned(16))) char lds[];
  const int tid = threadIdx.x, lane = tid & 63;
  const int wave = __builtin_amdgcn_readfirstlane(tid >> 6);
  const int c = lane & 15, g = lane >> 4;
  const int bid = blockIdx.x;
  const int xg = bid & 7, idx = bid >> 3;
  const int ks = xg >> 1, nt = (xg & 1) * 4 + (idx & 3), mt = idx >> 2;
  const int tile = mt * 8 + nt;
  RSTAMP(1,6); STAMP(1,0);

  f32x4 acc[4][8];
#pragma unroll
  for (int m = 0; m < 4; ++m)
#pragma unroll
    for (int j = 0; j < 8; ++j) acc[m][j] = (f32x4){0.f, 0.f, 0.f, 0.f};

  const int k0 = ks * 1024 + wave * 256;
  const char* Abase[4];
#pragma unroll
  for (int m = 0; m < 4; ++m) Abase[m] = ws + WS_O + ((size_t)(mt * 4 + ((wave + m) & 3)) * 128 + (k0 >> 5)) * 1024;
  const unsigned a_off = (unsigned)(lane * 16);
  const rsrc_t Bbase = make_rsrc((const char*)wo + (size_t)k0 * DM * 2 + nt * 128 * 2, 256 * DM * 2);
  const unsigned b_off = (unsigned)(g * 8 * DM * 2 + c * 16);

  constexpr int DEPTH = 4, NSTEP = 8;
  u32x4 sa[DEPTH][4], sb[DEPTH][8];
  auto load_stage = [&](int d, int s) {
#pragma unroll
    for (int m = 0; m < 4; ++m) sa[d][m] = *(const u32x4*)(Abase[m] + s * 1024 + a_off);
    load_b<DM>(sb[d], Bbase, b_off, s);
  };
#pragma unroll
  for (int s = 0; s < DEPTH; ++s) { load_stage(s, s); __builtin_amdgcn_sched_barrier(0); }
#pragma unroll
  for (int s = 0; s < NSTEP; ++s) {
    u32x4 bt[8], at[4];
    transpose8x8(sb[s % DEPTH], bt);
#pragma unroll
    for (int m = 0; m < 4; ++m) at[m] = sa[s % DEPTH][m];
    __builtin_amdgcn_sched_barrier(0);
    if (s==0) STAMP(1,1);
    if (s + DEPTH < NSTEP) load_stage(s % DEPTH, s + DEPTH);
    __builtin_amdgcn_sched_barrier(0);
    compute_stage(at, bt, acc);
    __builtin_amdgcn_sched_barrier(0);
  }

  STAMP(1,2);
  f32x4 r[8];
  reduce_waves<24576>(lds, acc, r, wave, lane);
  STAMP(1,3);

  const __amdgpu_buffer_rsrc_t rs = __builtin_amdgcn_make_buffer_rsrc(ws + WS_SLAB, 0, (int)SLAB_BYTES, 0x00020000);
  const int sl_off = (tile * 4 * 4 + wave) * 8192 + lane * 16;
#pragma unroll
  for (int j = 0; j < 8; ++j)
    __builtin_amdgcn_raw_buffer_store_b128(__builtin_bit_cast(u32x4, r[j]), rs, sl_off + ks * 32768 + j * 1024, 0, 16);
  asm volatile("s_waitcnt vmcnt(0)" ::: "memory");
  __syncthreads();
  unsigned* flag = (unsigned*)(lds + 4 * 24576);
  if (tid == 0) {
    const unsigned t = __hip_atomic_fetch_add((unsigned*)(ws + WS_CNT + tile * 256), 1u, __ATOMIC_RELAXED, __HIP_MEMORY_SCOPE_AGENT);
    *flag = t;
  }
  __syncthreads();
  STAMP(1,4); RSTAMP(1,7);
  if (*flag != 3u) return;
  __builtin_amdgcn_fence(__ATOMIC_ACQUIRE, "wavefront");
  f32x4 s0[8], s1[8], s2[8], s3[8];
#pragma unroll
  for (int j = 0; j < 8; ++j) {
    s0[j] = __builtin_bit_cast(f32x4, __builtin_amdgcn_raw_buffer_load_b128(rs, sl_off + 0 * 32768 + j * 1024, 0, 16));
    s1[j] = __builtin_bit_cast(f32x4, __builtin_amdgcn_raw_buffer_load_b128(rs, sl_off + 1 * 32768 + j * 1024, 0, 16));
    s2[j] = __builtin_bit_cast(f32x4, __builtin_amdgcn_raw_buffer_load_b128(rs, sl_off + 2 * 32768 + j * 1024, 0, 16));
    s3[j] = __builtin_bit_cast(f32x4, __builtin_amdgcn_raw_buffer_load_b128(rs, sl_off + 3 * 32768 + j * 1024, 0, 16));
  }
#pragma unroll
  for (int j = 0; j < 8; ++j) r[j] = (s0[j] + s1[j]) + (s2[j] + s3[j]);
  char* obase = (char*)out + (size_t)(mt * 64 + wave * 16 + 4 * g) * DM * 2 + nt * 256 + c * 16;
#pragma unroll
  for (int rr = 0; rr < 4; ++rr) {
    u32x4 v;
#pragma unroll
    for (int p = 0; p < 4; ++p) v[p] = pack2(r[2 * p][rr], r[2 * p + 1][rr]);
    *(u32x4*)(obase + (size_t)rr * DM * 2) = v;
  }
  DRAIN_STAMP(); STAMP(1,5); RSTAMP(1,7);
}

extern "C" void kernel_launch(void* const* d_in, const int* in_sizes, int n_in,
                              void* d_out, int out_size, void* d_ws, size_t ws_size,
                              hipStream_t stream) {
  const __hip_bfloat16* x  = (const __hip_bfloat16*)d_in[0];
  const __hip_bfloat16* kc = (const __hip_bfloat16*)d_in[1];
  const __hip_bfloat16* vc = (const __hip_bfloat16*)d_in[2];
  const __hip_bfloat16* wq = (const __hip_bfloat16*)d_in[3];
  const __hip_bfloat16* wo = (const __hip_bfloat16*)d_in[4];
  __hip_bfloat16* out = (__hip_bfloat16*)d_out;
  char* ws = (char*)d_ws;

  (void)hipFuncSetAttribute((const void*)k_qproj_attn, hipFuncAttributeMaxDynamicSharedMemorySize, K1_LDS);
  (void)hipFuncSetAttribute((const void*)k_oproj, hipFuncAttributeMaxDynamicSharedMemorySize, K2_LDS);
  k_qproj_attn<<<256, 256, K1_LDS, stream>>>(x, kc, vc, wq, ws);
  k_oproj<<<256, 256, K2_LDS, stream>>>(wo, ws, out);
}
```

```cpp
#define NT_WQ 0
#define NT_X 0
#define NT_KV 1
#define NT_WO 0
#include <hip/hip_runtime.h>
#include <hip/hip_bf16.h>
#include <stdint.h>

typedef short bf16x8 __attribute__((ext_vector_type(8)));
typedef float f32x4 __attribute__((ext_vector_type(4)));
typedef unsigned u32x4 __attribute__((ext_vector_type(4)));
typedef unsigned long long u64;

#define DI __device__ __forceinline__
#define STAMP(K,i)
#define RSTAMP(K,i)
#define DRAIN_STAMP()

constexpr int NB = 16, SQ = 32, DM = 1024, NH = 32, SK = 32, DH = 128;
constexpr int MROWS = NB * SQ;
constexpr int HD = NH * DH;

constexpr size_t WS_FLAG_O = 0;
constexpr size_t WS_FLAG_S = 256 * 128;
constexpr size_t WS_O = 2 * 256 * 128;
constexpr size_t O_BYTES = (size_t)MROWS * HD * 2;
constexpr size_t WS_SLAB = WS_O + O_BYTES;
constexpr size_t SLAB_BYTES = (size_t)64 * 4 * 32768;
constexpr u64 TAG = 0x9E3779B97F4A7C15ull;
constexpr unsigned SPIN_CAP = 1u << 20;

DI f32x4 mfma16(u32x4 a, u32x4 b, f32x4 c) {
  return __builtin_amdgcn_mfma_f32_16x16x32_bf16(__builtin_bit_cast(bf16x8, a), __builtin_bit_cast(bf16x8, b), c, 0, 0, 0);
}
typedef __bf16 bf16x2_t __attribute__((ext_vector_type(2)));
typedef float f32x2 __attribute__((ext_vector_type(2)));
DI unsigned pack2(float lo, float hi) {
  f32x2 f = {lo, hi};
  return __builtin_bit_cast(unsigned, __builtin_convertvector(f, bf16x2_t));
}
DI float bf16_round(float f) { return __uint_as_float(pack2(f, 0.f) << 16); }

DI void transpose8x8(const u32x4 (&raw)[8], u32x4 (&out)[8]) {
#pragma unroll
  for (int d = 0; d < 4; ++d) {
#pragma unroll
    for (int p = 0; p < 4; ++p) {
      out[2 * d][p]     = __builtin_amdgcn_perm(raw[2 * p + 1][d], raw[2 * p][d], 0x05040100u);
      out[2 * d + 1][p] = __builtin_amdgcn_perm(raw[2 * p + 1][d], raw[2 * p][d], 0x07060302u);
    }
  }
}

template <int NT>
DI void glds16(const char* sbase, unsigned voff, unsigned lds_dst) {
  unsigned keep;
  if (NT)
    asm volatile("s_mov_b32 %0, m0\n\ts_mov_b32 m0, %3\n\ts_nop 0\n\tglobal_load_lds_dwordx4 %1, %2 nt\n\ts_mov_b32 m0, %0"
                 : "=&s"(keep) : "v"(voff), "s"(sbase), "s"(lds_dst) : "memory");
  else
    asm volatile("s_mov_b32 %0, m0\n\ts_mov_b32 m0, %3\n\ts_nop 0\n\tglobal_load_lds_dwordx4 %1, %2\n\ts_mov_b32 m0, %0"
                 : "=&s"(keep) : "v"(voff), "s"(sbase), "s"(lds_dst) : "memory");
}

typedef __amdgpu_buffer_rsrc_t rsrc_t;
DI rsrc_t make_rsrc(const void* p, unsigned bytes) { return __builtin_amdgcn_make_buffer_rsrc((void*)p, 0, (int)bytes, 0x00020000); }
template <int NT> DI u32x4 bload(rsrc_t rs, unsigned voff, unsigned soff) { return __builtin_amdgcn_raw_buffer_load_b128(rs, (int)voff, (int)soff, NT ? 2 : 0); }
DI u32x4 bload_sc1(rsrc_t rs, unsigned voff, unsigned soff) { return __builtin_amdgcn_raw_buffer_load_b128(rs, (int)voff, (int)soff, 16); }
DI void bstore_sc1(u32x4 v, rsrc_t rs, unsigned voff, unsigned soff) { __builtin_amdgcn_raw_buffer_store_b128(v, rs, (int)voff, (int)soff, 16); }

DI void compute_stage(const u32x4 (&sa)[4], const u32x4 (&bt)[8], f32x4 (&acc)[4][8]) {
#pragma unroll
  for (int j = 0; j < 8; ++j)
#pragma unroll
    for (int m = 0; m < 4; ++m) acc[m][j] = mfma16(sa[m], bt[j], acc[m][j]);
}

DI void reduce_waves(char* lds, const f32x4 (&acc)[4][8], f32x4 (&res)[8], int wave, int lane) {
#pragma unroll
  for (int m = 1; m < 4; ++m)
#pragma unroll
    for (int j = 0; j < 8; ++j) *(f32x4*)(lds + wave * 32768 + ((m - 1) * 8 + j) * 1024 + lane * 16) = acc[m][j];
  __syncthreads();
#pragma unroll
  for (int j = 0; j < 8; ++j) res[j] = acc[0][j];
#pragma unroll
  for (int m = 1; m < 4; ++m) {
    const int w = (wave - m) & 3;
#pragma unroll
    for (int j = 0; j < 8; ++j) res[j] += *(const f32x4*)(lds + w * 32768 + ((m - 1) * 8 + j) * 1024 + lane * 16);
  }
}

#define INTERLEAVE_STEP()                                                                   \
  _Pragma("unroll") for (int i_ = 0; i_ < 32; ++i_) {                                       \
    __builtin_amdgcn_sched_group_barrier(0x008, 1, 0);                                      \
    if (i_ < 24 && (i_ & 1)) __builtin_amdgcn_sched_group_barrier(0x020, 1, 0);             \
    if (i_ >= 8 && i_ < 20) __builtin_amdgcn_sched_group_barrier(0x100, 1, 0);              \
    if (i_ >= 16) __builtin_amdgcn_sched_group_barrier(0x002, 2, 0);                        \
  }

constexpr int QROW = 272;
constexpr int LDS_BYTES = 5 * 32768;

__global__ __launch_bounds__(256, 1) void k_fused(const __hip_bfloat16* __restrict__ x,
                                                  const __hip_bfloat16* __restrict__ kc,
                                                  const __hip_bfloat16* __restrict__ vc,
                                                  const __hip_bfloat16* __restrict__ wq,
                                                  const __hip_bfloat16* __restrict__ wo,
                                                  char* __restrict__ ws,
                                                  __hip_bfloat16* __restrict__ out) {
  extern __shared__ __attribute__((aligned(16))) char lds[];
  const int tid = threadIdx.x, lane = tid & 63;
  const int wave = __builtin_amdgcn_readfirstlane(tid >> 6);
  const int c = lane & 15, g = lane >> 4;
  const int bid = blockIdx.x;
  const int xg = bid & 7, idx = bid >> 3;
  const int mt = idx >> 2;
  const int h = xg + 8 * (idx & 3);
  const int ks = xg >> 1, nt = (xg & 1) * 4 + (idx & 3);
  RSTAMP(0,6); STAMP(0,0);

  char* abuf = lds + wave * 32768;
  const unsigned abuf_l = (unsigned)(uintptr_t)(__attribute__((address_space(3))) char*)abuf;
  f32x4 acc[4][8];
#pragma unroll
  for (int m = 0; m < 4; ++m)
#pragma unroll
    for (int j = 0; j < 8; ++j) acc[m][j] = (f32x4){0.f, 0.f, 0.f, 0.f};

  const char* xrow = (const char*)x + (size_t)(mt * 64) * (DM * 2) + wave * 512;
  auto dma_x = [&](int hh, int i) {
    const int row = 4 * i + g;
    const unsigned off = (unsigned)(row * (DM * 2) + hh * 256 + ((c ^ (row & 15)) << 4));
    glds16<NT_X>(xrow, off, abuf_l + hh * 16384 + i * 1024);
  };
  const rsrc_t rq = make_rsrc((const char*)wq + (size_t)(wave * 256) * HD * 2 + h * DH * 2, 256 * HD * 2);
  const unsigned bq_off = (unsigned)(g * 8 * HD * 2 + c * 16);
  auto load_wq = [&](u32x4 (&sb)[8], int step) {
#pragma unroll
    for (int i = 0; i < 8; ++i) sb[i] = bload<NT_WQ>(rq, bq_off, (unsigned)((step * 32 + i) * HD * 2));
  };
  const int b = mt * 2 + (wave >> 1);
  const char* kbase = (const char*)kc + (size_t)(b * NH + h) * SK * DH * 2;
  const char* vbase = (const char*)vc + (size_t)(b * NH + h) * SK * DH * 2;
  char* kvbuf = lds + 4 * 32768 + (wave >> 1) * 16384;
  auto dma_kv = [&]() {
    const unsigned kv_l = (unsigned)(uintptr_t)(__attribute__((address_space(3))) char*)kvbuf;
    if (wave & 1) {
#pragma unroll
      for (int e = 0; e < 8; ++e) glds16<NT_KV>(vbase, (unsigned)((16 * (e >> 2) + 4 * g + (e & 3)) * 256 + c * 16), kv_l + 8192 + e * 1024);
    } else {
#pragma unroll
      for (int e = 0; e < 8; ++e) glds16<NT_KV>(kbase, (unsigned)(((e >> 2) * 16 + c) * 256 + (e & 3) * 64 + g * 16), kv_l + e * 1024);
    }
  };

  const int k0 = ks * 1024 + wave * 256;
  const char* wo_base = (const char*)wo + (size_t)k0 * DM * 2 + nt * 256;
  const rsrc_t ro = make_rsrc(wo_base, 256 * DM * 2);
  const unsigned bo_off = (unsigned)(g * 32 * DM * 2 + c * 16);
  u32x4 so[4][8];
  const int rot = mt;

  {
    constexpr int DEPTH = 3, NSTEP = 8;
    u32x4 sb[DEPTH][8];
#pragma unroll
    for (int i = 0; i < 16; ++i) dma_x(rot >> 2, i);
    load_wq(sb[0], 0 ^ rot);
    load_wq(sb[1], 1 ^ rot);
    __builtin_amdgcn_sched_barrier(0);
    unsigned aoff[4];
#pragma unroll
    for (int s = 0; s < 4; ++s) aoff[s] = (unsigned)(c * 256 + (((4 * (s ^ (rot & 3)) + g) ^ c) << 4));
    auto read_a = [&](u32x4 (&at)[4], int s) {
#pragma unroll
      for (int m = 0; m < 4; ++m)
        at[m] = *(const u32x4*)(abuf + ((wave + m) & 3) * 4096 + ((s >> 2) ^ (rot >> 2)) * 16384 + aoff[s & 3]);
    };
    u32x4 at[2][4], bt[2][8];
    asm volatile("s_waitcnt vmcnt(16)" ::: "memory");
    read_a(at[0], 0);
    transpose8x8(sb[0], bt[0]);
    __builtin_amdgcn_sched_barrier(0);
    STAMP(0,1);
#pragma unroll
    for (int s = 0; s < NSTEP; ++s) {
      if (s < 3) {
#pragma unroll
        for (int i = 0; i < (s == 2 ? 6 : 5); ++i) dma_x(1 ^ (rot >> 2), s * 5 + i);
      }
      if (s + 2 < NSTEP) load_wq(sb[(s + 2) % DEPTH], (s + 2) ^ rot);
      if (s == 5) dma_kv();
      if (s == 3) asm volatile("s_waitcnt vmcnt(16)" ::: "memory");
      if (s + 1 < NSTEP) { read_a(at[(s + 1) & 1], s + 1); transpose8x8(sb[(s + 1) % DEPTH], bt[(s + 1) & 1]); }
      compute_stage(at[s & 1], bt[s & 1], acc);
      INTERLEAVE_STEP();
      __builtin_amdgcn_sched_barrier(0);
    }
  }
  STAMP(0,2);
  asm volatile("s_waitcnt vmcnt(0)" ::: "memory");
#define PF(a, n) do { __builtin_amdgcn_sched_barrier(0); _Pragma("unroll") for (int u_ = (a); u_ < (a) + (n); ++u_) { \
    const int ta_ = (u_ >> 3) ^ rot; \
    so[u_ >> 3][u_ & 7] = bload<NT_WO>(ro, bo_off, (unsigned)((128 * (ta_ >> 2) + 8 * (ta_ & 3) + (u_ & 7)) * DM * 2)); } \
    __builtin_amdgcn_sched_barrier(0); } while (0)
  {
    f32x4 q[8];
    PF(0, 3);
#pragma unroll
    for (int m = 1; m < 4; ++m) {
#pragma unroll
      for (int j = 0; j < 8; ++j) *(f32x4*)(lds + wave * 32768 + ((m - 1) * 8 + j) * 1024 + lane * 16) = acc[m][j];
      if (m == 1) PF(3, 2); else if (m == 2) PF(5, 2); else PF(7, 2);
    }
    __syncthreads();
#pragma unroll
    for (int j = 0; j < 8; ++j) q[j] = acc[0][j];
#pragma unroll
    for (int m = 1; m < 4; ++m) {
      const int w = (wave - m) & 3;
#pragma unroll
      for (int j = 0; j < 8; ++j) q[j] += *(const f32x4*)(lds + w * 32768 + ((m - 1) * 8 + j) * 1024 + lane * 16);
      if (m == 1) PF(9, 2); else if (m == 2) PF(11, 2); else PF(13, 2);
    }
    STAMP(0,3);
    char* qimg = abuf + 24 * 1024;
#pragma unroll
    for (int r = 0; r < 4; ++r) {
      u32x4 v;
#pragma unroll
      for (int p = 0; p < 4; ++p) v[p] = pack2(q[2 * p][r], q[2 * p + 1][r]);
      *(u32x4*)(qimg + (4 * g + r) * QROW + c * 16) = v;
    }
    __builtin_amdgcn_wave_barrier();
    PF(15, 3);
    u32x4 qf[4];
#pragma unroll
    for (int s = 0; s < 4; ++s) qf[s] = *(const u32x4*)(qimg + c * QROW + s * 64 + g * 16);
    u32x4 kf[2][4], vraw[8];
#pragma unroll
    for (int e = 0; e < 8; ++e) kf[e >> 2][e & 3] = *(const u32x4*)(kvbuf + e * 1024 + lane * 16);
#pragma unroll
    for (int e = 0; e < 8; ++e) vraw[e] = *(const u32x4*)(kvbuf + 8192 + e * 1024 + lane * 16);

    f32x4 sacc[2];
#pragma unroll
    for (int kt = 0; kt < 2; ++kt) {
      sacc[kt] = (f32x4){0.f, 0.f, 0.f, 0.f};
#pragma unroll
      for (int s = 0; s < 4; ++s) sacc[kt] = mfma16(kf[kt][s], qf[s], sacc[kt]);
    }
    PF(18, 3);
    const float scale = 0.08838834764831845f;
    float sv[8];
#pragma unroll
    for (int e = 0; e < 8; ++e) sv[e] = bf16_round(bf16_round(sacc[e >> 2][e & 3]) * scale);
    float mx = sv[0];
#pragma unroll
    for (int e = 1; e < 8; ++e) mx = fmaxf(mx, sv[e]);
    PF(21, 3);
    mx = fmaxf(mx, __shfl_xor(mx, 16));
    mx = fmaxf(mx, __shfl_xor(mx, 32));
    PF(24, 3);
    float sum = 0.f;
#pragma unroll
    for (int e = 0; e < 8; ++e) { sv[e] = __expf(sv[e] - mx); sum += sv[e]; }
    PF(27, 3);
    sum += __shfl_xor(sum, 16);
    sum += __shfl_xor(sum, 32);
    PF(30, 2);
    const float inv = 1.0f / sum;
    u32x4 pf;
#pragma unroll
    for (int p = 0; p < 4; ++p) pf[p] = pack2(sv[2 * p] * inv, sv[2 * p + 1] * inv);

    u32x4 vt[8];
    transpose8x8(vraw, vt);
    f32x4 oacc[8];
#pragma unroll
    for (int j = 0; j < 8; ++j) oacc[j] = mfma16(vt[j], pf, (f32x4){0.f, 0.f, 0.f, 0.f});

    const rsrc_t rO = make_rsrc(ws + WS_O, (unsigned)O_BYTES);
#pragma unroll
    for (int r = 0; r < 4; ++r) {
      u32x4 v;
#pragma unroll
      for (int p = 0; p < 4; ++p) v[p] = pack2(oacc[2 * p][r], oacc[2 * p + 1][r]);
      bstore_sc1(v, rO, (unsigned)(lane * 16), (unsigned)(((mt * 4 + wave) * 128 + h * 4 + r) * 1024));
    }
  }
  STAMP(0,4);
  asm volatile("s_waitcnt vmcnt(0)" ::: "memory");
  __syncthreads();
  if (wave == 0 && lane < 8) {
    const int cb = (mt * 4 + (lane & 3)) * 8 + (h >> 3) * 2 + (lane >> 2);
    __hip_atomic_store((u64*)(ws + WS_FLAG_O + cb * 128 + (h & 7) * 8), TAG, __ATOMIC_RELAXED, __HIP_MEMORY_SCOPE_AGENT);
  }
  STAMP(0,5);

#pragma unroll
  for (int t = 4; t < 8; ++t)
#pragma unroll
    for (int i = 0; i < 8; ++i)
      glds16<NT_WO>(wo_base, bo_off + (unsigned)((128 * ((t ^ rot) >> 2) + 8 * ((t ^ rot) & 3) + i) * DM * 2), abuf_l + ((t - 4) * 8 + i) * 1024);

  if (wave == 0) {
    u64* fl = (u64*)(ws + WS_FLAG_O + bid * 128) + (lane & 7);
    for (unsigned spin = 0; spin < SPIN_CAP; ++spin) {
      const u64 v = __hip_atomic_load(fl, __ATOMIC_RELAXED, __HIP_MEMORY_SCOPE_AGENT);
      if (__all(v == TAG)) break;
      __builtin_amdgcn_s_sleep(2);
    }
    if (lane < 8) __hip_atomic_store(fl, 0ull, __ATOMIC_RELAXED, __HIP_MEMORY_SCOPE_AGENT);
  }
  __syncthreads();
  __builtin_amdgcn_fence(__ATOMIC_ACQUIRE, "wavefront");
  STAMP(1,0);
#pragma unroll
  for (int m = 0; m < 4; ++m)
#pragma unroll
    for (int j = 0; j < 8; ++j) acc[m][j] = (f32x4){0.f, 0.f, 0.f, 0.f};
  {
    const rsrc_t rA = make_rsrc(ws + WS_O, (unsigned)O_BYTES);
    unsigned a_soff[4];
#pragma unroll
    for (int m = 0; m < 4; ++m) a_soff[m] = (unsigned)(((mt * 4 + ((wave + m) & 3)) * 128 + (k0 >> 5)) * 1024);
    constexpr int DA = 4, NSTEP = 8;
    u32x4 sa[DA][4];
    auto load_a = [&](int d, int t) {
#pragma unroll
      for (int m = 0; m < 4; ++m) sa[d][m] = bload_sc1(rA, (unsigned)(lane * 16), a_soff[m] + (t ^ rot) * 1024);
    };
#pragma unroll
    for (int t = 0; t < DA; ++t) load_a(t, t);
    u32x4 bt[2][8];
    transpose8x8(so[0], bt[0]);
    __builtin_amdgcn_sched_barrier(0);
#pragma unroll
    for (int t = 0; t < NSTEP; ++t) {
      u32x4 at[4];
#pragma unroll
      for (int m = 0; m < 4; ++m) at[m] = sa[t % DA][m];
      __builtin_amdgcn_sched_barrier(0);
      if (t == 0) STAMP(1,1);
      if (t + DA < NSTEP) load_a(t % DA, t + DA);
      if (t + 1 < 4) transpose8x8(so[t + 1], bt[(t + 1) & 1]);
      else if (t + 1 < NSTEP) {
        u32x4 raw[8];
#pragma unroll
        for (int i = 0; i < 8; ++i) raw[i] = *(const u32x4*)(abuf + ((t + 1 - 4) * 8 + i) * 1024 + lane * 16);
        transpose8x8(raw, bt[(t + 1) & 1]);
      }
      compute_stage(at, bt[t & 1], acc);
      INTERLEAVE_STEP();
      __builtin_amdgcn_sched_barrier(0);
    }
  }
  STAMP(1,2);
  f32x4 r[8];
  reduce_waves(lds, acc, r, wave, lane);
  STAMP(1,3);

  const int tile = mt * 8 + nt;
  const rsrc_t rs = make_rsrc(ws + WS_SLAB, (unsigned)SLAB_BYTES);
  const unsigned sl_off = (unsigned)((tile * 16 + wave) * 8192 + lane * 16);
#pragma unroll
  for (int j = 0; j < 8; ++j) bstore_sc1(__builtin_bit_cast(u32x4, r[j]), rs, sl_off, (unsigned)(ks * 32768 + j * 1024));
  asm volatile("s_waitcnt vmcnt(0)" ::: "memory");
  __syncthreads();
  if (wave == 0 && lane < 4) {
    const int ob = (mt * 4 + (nt & 3)) * 8 + lane * 2 + (nt >> 2);
    __hip_atomic_store((u64*)(ws + WS_FLAG_S + ob * 128 + ks * 8), TAG, __ATOMIC_RELAXED, __HIP_MEMORY_SCOPE_AGENT);
  }
  STAMP(1,4);
  if (wave != ks) { RSTAMP(1,7); return; }
  {
    u64* fl = (u64*)(ws + WS_FLAG_S + bid * 128) + (lane & 3);
    for (unsigned spin = 0; spin < SPIN_CAP; ++spin) {
      const u64 v = __hip_atomic_load(fl, __ATOMIC_RELAXED, __HIP_MEMORY_SCOPE_AGENT);
      if (__all(v == TAG)) break;
      __builtin_amdgcn_s_sleep(1);
    }
    if (lane < 4) __hip_atomic_store(fl, 0ull, __ATOMIC_RELAXED, __HIP_MEMORY_SCOPE_AGENT);
  }
  __builtin_amdgcn_fence(__ATOMIC_ACQUIRE, "wavefront");
  f32x4 s0[8], s1[8], s2[8], s3[8];
#pragma unroll
  for (int j = 0; j < 8; ++j) {
    s0[j] = __builtin_bit_cast(f32x4, bload_sc1(rs, sl_off, (unsigned)(0 * 32768 + j * 1024)));
    s1[j] = __builtin_bit_cast(f32x4, bload_sc1(rs, sl_off, (unsigned)(1 * 32768 + j * 1024)));
    s2[j] = __builtin_bit_cast(f32x4, bload_sc1(rs, sl_off, (unsigned)(2 * 32768 + j * 1024)));
    s3[j] = __builtin_bit_cast(f32x4, bload_sc1(rs, sl_off, (unsigned)(3 * 32768 + j * 1024)));
  }
#pragma unroll
  for (int j = 0; j < 8; ++j) r[j] = (s0[j] + s1[j]) + (s2[j] + s3[j]);
  char* obase = (char*)out + (size_t)(mt * 64 + wave * 16 + 4 * g) * DM * 2 + nt * 256 + c * 16;
#pragma unroll
  for (int rr = 0; rr < 4; ++rr) {
    u32x4 v;
#pragma unroll
    for (int p = 0; p < 4; ++p) v[p] = pack2(r[2 * p][rr], r[2 * p + 1][rr]);
    *(u32x4*)(obase + (size_t)rr * DM * 2) = v;
  }
  DRAIN_STAMP(); STAMP(1,5); RSTAMP(1,7);
}

extern "C" void kernel_launch(void* const* d_in, const int* in_sizes, int n_in,
                              void* d_out, int out_size, void* d_ws, size_t ws_size,
                              hipStream_t stream) {
  const __hip_bfloat16* x  = (const __hip_bfloat16*)d_in[0];
  const __hip_bfloat16* kc = (const __hip_bfloat16*)d_in[1];
  const __hip_bfloat16* vc = (const __hip_bfloat16*)d_in[2];
  const __hip_bfloat16* wq = (const __hip_bfloat16*)d_in[3];
  const __hip_bfloat16* wo = (const __hip_bfloat16*)d_in[4];
  __hip_bfloat16* out = (__hip_bfloat16*)d_out;
  char* ws = (char*)d_ws;
  (void)hipFuncSetAttribute((const void*)k_fused, hipFuncAttributeMaxDynamicSharedMemorySize, LDS_BYTES);
  k_fused<<<256, 256, LDS_BYTES, stream>>>(x, kc, vc, wq, wo, ws, out);
}
```

```cpp
#define NT_WQ 0
#define NT_X 0
#define NT_KV 0
#define NT_WO 0
#include <hip/hip_runtime.h>
#include <hip/hip_bf16.h>
#include <stdint.h>

typedef short bf16x8 __attribute__((ext_vector_type(8)));
typedef float f32x4 __attribute__((ext_vector_type(4)));
typedef unsigned u32x4 __attribute__((ext_vector_type(4)));
typedef unsigned long long u64;

#define DI __device__ __forceinline__
#define STAMP(K,i)
#define RSTAMP(K,i)
#define DRAIN_STAMP()

constexpr int NB = 16, SQ = 32, DM = 1024, NH = 32, SK = 32, DH = 128;
constexpr int MROWS = NB * SQ;
constexpr int HD = NH * DH;

constexpr size_t WS_FLAG_O = 0;
constexpr size_t WS_FLAG_S = 256 * 128;
constexpr size_t WS_O = 2 * 256 * 128;
constexpr size_t O_BYTES = (size_t)MROWS * HD * 2;
constexpr size_t WS_SLAB = WS_O + O_BYTES;
constexpr size_t SLAB_BYTES = (size_t)64 * 4 * 32768;
constexpr u64 TAG = 0x9E3779B97F4A7C15ull;
constexpr unsigned SPIN_CAP = 1u << 20;

DI f32x4 mfma16(u32x4 a, u32x4 b, f32x4 c) {
  return __builtin_amdgcn_mfma_f32_16x16x32_bf16(__builtin_bit_cast(bf16x8, a), __builtin_bit_cast(bf16x8, b), c, 0, 0, 0);
}
typedef __bf16 bf16x2_t __attribute__((ext_vector_type(2)));
typedef float f32x2 __attribute__((ext_vector_type(2)));
DI unsigned pack2(float lo, float hi) {
  f32x2 f = {lo, hi};
  return __builtin_bit_cast(unsigned, __builtin_convertvector(f, bf16x2_t));
}
DI float bf16_round(float f) { return __uint_as_float(pack2(f, 0.f) << 16); }

DI void transpose8x8(const u32x4 (&raw)[8], u32x4 (&out)[8]) {
#pragma unroll
  for (int d = 0; d < 4; ++d) {
#pragma unroll
    for (int p = 0; p < 4; ++p) {
      out[2 * d][p]     = __builtin_amdgcn_perm(raw[2 * p + 1][d], raw[2 * p][d], 0x05040100u);
      out[2 * d + 1][p] = __builtin_amdgcn_perm(raw[2 * p + 1][d], raw[2 * p][d], 0x07060302u);
    }
  }
}

template <int NT>
DI void glds16(const char* sbase, unsigned voff, unsigned lds_dst) {
  unsigned keep;
  if (NT)
    asm volatile("s_mov_b32 %0, m0\n\ts_mov_b32 m0, %3\n\ts_nop 0\n\tglobal_load_lds_dwordx4 %1, %2 nt\n\ts_mov_b32 m0, %0"
                 : "=&s"(keep) : "v"(voff), "s"(sbase), "s"(lds_dst) : "memory");
  else
    asm volatile("s_mov_b32 %0, m0\n\ts_mov_b32 m0, %3\n\ts_nop 0\n\tglobal_load_lds_dwordx4 %1, %2\n\ts_mov_b32 m0, %0"
                 : "=&s"(keep) : "v"(voff), "s"(sbase), "s"(lds_dst) : "memory");
}

typedef __amdgpu_buffer_rsrc_t rsrc_t;
DI rsrc_t make_rsrc(const void* p, unsigned bytes) { return __builtin_amdgcn_make_buffer_rsrc((void*)p, 0, (int)bytes, 0x00020000); }
template <int NT> DI u32x4 bload(rsrc_t rs, unsigned voff, unsigned soff) { return __builtin_amdgcn_raw_buffer_load_b128(rs, (int)voff, (int)soff, NT ? 2 : 0); }
DI u32x4 bload_sc1(rsrc_t rs, unsigned voff, unsigned soff) { return __builtin_amdgcn_raw_buffer_load_b128(rs, (int)voff, (int)soff, 16); }
DI void bstore_sc1(u32x4 v, rsrc_t rs, unsigned voff, unsigned soff) { __builtin_amdgcn_raw_buffer_store_b128(v, rs, (int)voff, (int)soff, 16); }

DI void compute_stage(const u32x4 (&sa)[4], const u32x4 (&bt)[8], f32x4 (&acc)[4][8]) {
#pragma unroll
  for (int j = 0; j < 8; ++j)
#pragma unroll
    for (int m = 0; m < 4; ++m) acc[m][j] = mfma16(sa[m], bt[j], acc[m][j]);
}

DI void reduce_waves(char* lds, const f32x4 (&acc)[4][8], f32x4 (&res)[8], int wave, int lane) {
#pragma unroll
  for (int m = 1; m < 4; ++m)
#pragma unroll
    for (int j = 0; j < 8; ++j) *(f32x4*)(lds + wave * 32768 + ((m - 1) * 8 + j) * 1024 + lane * 16) = acc[m][j];
  __syncthreads();
#pragma unroll
  for (int j = 0; j < 8; ++j) res[j] = acc[0][j];
#pragma unroll
  for (int m = 1; m < 4; ++m) {
    const int w = (wave - m) & 3;
#pragma unroll
    for (int j = 0; j < 8; ++j) res[j] += *(const f32x4*)(lds + w * 32768 + ((m - 1) * 8 + j) * 1024 + lane * 16);
  }
}

#define INTERLEAVE_STEP()                                                                   \
  _Pragma("unroll") for (int i_ = 0; i_ < 32; ++i_) {                                       \
    __builtin_amdgcn_sched_group_barrier(0x008, 1, 0);                                      \
    if (i_ < 24 && (i_ & 1)) __builtin_amdgcn_sched_group_barrier(0x020, 1, 0);             \
    if (i_ >= 8 && i_ < 20) __builtin_amdgcn_sched_group_barrier(0x100, 1, 0);              \
    if (i_ >= 16) __builtin_amdgcn_sched_group_barrier(0x002, 2, 0);                        \
  }

constexpr int QROW = 272;
constexpr int LDS_BYTES = 5 * 32768;

__global__ __launch_bounds__(256, 1) void k_fused(const __hip_bfloat16* __restrict__ x,
                                                  const __hip_bfloat16* __restrict__ kc,
                                                  const __hip_bfloat16* __restrict__ vc,
                                                  const __hip_bfloat16* __restrict__ wq,
                                                  const __hip_bfloat16* __restrict__ wo,
                                                  char* __restrict__ ws,
                                                  __hip_bfloat16* __restrict__ out) {
  extern __shared__ __attribute__((aligned(16))) char lds[];
  const int tid = threadIdx.x, lane = tid & 63;
  const int wave = __builtin_amdgcn_readfirstlane(tid >> 6);
  const int c = lane & 15, g = lane >> 4;
  const int bid = blockIdx.x;
  const int xg = bid & 7, idx = bid >> 3;
  const int mt = idx >> 2;
  const int h = xg + 8 * (idx & 3);
  const int ks = xg >> 1, nt = (xg & 1) * 4 + (idx & 3);
  RSTAMP(0,6); STAMP(0,0);

  char* abuf = lds + wave * 32768;
  const unsigned abuf_l = (unsigned)(uintptr_t)(__attribute__((address_space(3))) char*)abuf;
  f32x4 acc[4][8];
#pragma unroll
  for (int m = 0; m < 4; ++m)
#pragma unroll
    for (int j = 0; j < 8; ++j) acc[m][j] = (f32x4){0.f, 0.f, 0.f, 0.f};

  const char* xrow = (const char*)x + (size_t)(mt * 64) * (DM * 2) + wave * 512;
  auto dma_x = [&](int hh, int i) {
    const int row = 4 * i + g;
    const unsigned off = (unsigned)(row * (DM * 2) + hh * 256 + ((c ^ (row & 15)) << 4));
    glds16<NT_X>(xrow, off, abuf_l + hh * 16384 + i * 1024);
  };
  const rsrc_t rq = make_rsrc((const char*)wq + (size_t)(wave * 256) * HD * 2 + h * DH * 2, 256 * HD * 2);
  const unsigned bq_off = (unsigned)(g * 8 * HD * 2 + c * 16);
  auto load_wq = [&](u32x4 (&sb)[8], int step) {
#pragma unroll
    for (int i = 0; i < 8; ++i) sb[i] = bload<NT_WQ>(rq, bq_off, (unsigned)((step * 32 + i) * HD * 2));
  };
  const int b = mt * 2 + (wave >> 1);
  const char* kbase = (const char*)kc + (size_t)(b * NH + h) * SK * DH * 2;
  const char* vbase = (const char*)vc + (size_t)(b * NH + h) * SK * DH * 2;
  char* kvbuf = lds + 4 * 32768 + (wave >> 1) * 16384;
  auto dma_kv = [&]() {
    const unsigned kv_l = (unsigned)(uintptr_t)(__attribute__((address_space(3))) char*)kvbuf;
    if (wave & 1) {
#pragma unroll
      for (int e = 0; e < 8; ++e) glds16<NT_KV>(vbase, (unsigned)((16 * (e >> 2) + 4 * g + (e & 3)) * 256 + c * 16), kv_l + 8192 + e * 1024);
    } else {
#pragma unroll
      for (int e = 0; e < 8; ++e) glds16<NT_KV>(kbase, (unsigned)(((e >> 2) * 16 + c) * 256 + (e & 3) * 64 + g * 16), kv_l + e * 1024);
    }
  };

  const int k0 = ks * 1024 + wave * 256;
  const char* wo_base = (const char*)wo + (size_t)k0 * DM * 2 + nt * 256;
  const rsrc_t ro = make_rsrc(wo_base, 256 * DM * 2);
  const unsigned bo_off = (unsigned)(g * 32 * DM * 2 + c * 16);
  u32x4 so[4][8];
  const int rot = mt;

  {
    constexpr int DEPTH = 4, NSTEP = 8;
    u32x4 sb[DEPTH][8];
#pragma unroll
    for (int i = 0; i < 16; ++i) dma_x(rot >> 2, i);
    load_wq(sb[0], 0 ^ rot);
    load_wq(sb[1], 1 ^ rot);
    load_wq(sb[2], 2 ^ rot);
    __builtin_amdgcn_sched_barrier(0);
    unsigned aoff[4];
#pragma unroll
    for (int s = 0; s < 4; ++s) aoff[s] = (unsigned)(c * 256 + (((4 * (s ^ (rot & 3)) + g) ^ c) << 4));
    auto read_a = [&](u32x4 (&at)[4], int s) {
#pragma unroll
      for (int m = 0; m < 4; ++m)
        at[m] = *(const u32x4*)(abuf + ((wave + m) & 3) * 4096 + ((s >> 2) ^ (rot >> 2)) * 16384 + aoff[s & 3]);
    };
    u32x4 at[2][4], bt[2][8];
    asm volatile("s_waitcnt vmcnt(24)" ::: "memory");
    read_a(at[0], 0);
    transpose8x8(sb[0], bt[0]);
    __builtin_amdgcn_sched_barrier(0);
    STAMP(0,1);
#pragma unroll
    for (int s = 0; s < NSTEP; ++s) {
      if (s < 3) {
#pragma unroll
        for (int i = 0; i < (s == 2 ? 6 : 5); ++i) dma_x(1 ^ (rot >> 2), s * 5 + i);
      }
      if (s + 3 < NSTEP) load_wq(sb[(s + 3) % DEPTH], (s + 3) ^ rot);
      if (s == 4) dma_kv();
      if (s == 3) asm volatile("s_waitcnt vmcnt(16)" ::: "memory");
      if (s + 1 < NSTEP) { read_a(at[(s + 1) & 1], s + 1); transpose8x8(sb[(s + 1) % DEPTH], bt[(s + 1) & 1]); }
      compute_stage(at[s & 1], bt[s & 1], acc);
      INTERLEAVE_STEP();
      __builtin_amdgcn_sched_barrier(0);
    }
  }
  STAMP(0,2);
  asm volatile("s_waitcnt vmcnt(0)" ::: "memory");
#define PF(a, n) do { __builtin_amdgcn_sched_barrier(0); _Pragma("unroll") for (int u_ = (a); u_ < (a) + (n); ++u_) { \
    const int ta_ = (u_ >> 3) ^ rot; \
    so[u_ >> 3][u_ & 7] = bload<NT_WO>(ro, bo_off, (unsigned)((128 * (ta_ >> 2) + 8 * (ta_ & 3) + (u_ & 7)) * DM * 2)); } \
    __builtin_amdgcn_sched_barrier(0); } while (0)
  {
    f32x4 q[8];
    PF(0, 3);
#pragma unroll
    for (int m = 1; m < 4; ++m) {
#pragma unroll
      for (int j = 0; j < 8; ++j) *(f32x4*)(lds + wave * 32768 + ((m - 1) * 8 + j) * 1024 + lane * 16) = acc[m][j];
      if (m == 1) PF(3, 2); else if (m == 2) PF(5, 2); else PF(7, 2);
    }
    __syncthreads();
#pragma unroll
    for (int j = 0; j < 8; ++j) q[j] = acc[0][j];
#pragma unroll
    for (int m = 1; m < 4; ++m) {
      const int w = (wave - m) & 3;
#pragma unroll
      for (int j = 0; j < 8; ++j) q[j] += *(const f32x4*)(lds + w * 32768 + ((m - 1) * 8 + j) * 1024 + lane * 16);
      if (m == 1) PF(9, 2); else if (m == 2) PF(11, 2); else PF(13, 2);
    }
    STAMP(0,3);
    char* qimg = abuf + 24 * 1024;
#pragma unroll
    for (int r = 0; r < 4; ++r) {
      u32x4 v;
#pragma unroll
      for (int p = 0; p < 4; ++p) v[p] = pack2(q[2 * p][r], q[2 * p + 1][r]);
      *(u32x4*)(qimg + (4 * g + r) * QROW + c * 16) = v;
    }
    __builtin_amdgcn_wave_barrier();
    PF(15, 3);
    u32x4 qf[4];
#pragma unroll
    for (int s = 0; s < 4; ++s) qf[s] = *(const u32x4*)(qimg + c * QROW + s * 64 + g * 16);
    u32x4 kf[2][4], vraw[8];
#pragma unroll
    for (int e = 0; e < 8; ++e) kf[e >> 2][e & 3] = *(const u32x4*)(kvbuf + e * 1024 + lane * 16);
#pragma unroll
    for (int e = 0; e < 8; ++e) vraw[e] = *(const u32x4*)(kvbuf + 8192 + e * 1024 + lane * 16);

    f32x4 sacc[2];
#pragma unroll
    for (int kt = 0; kt < 2; ++kt) {
      sacc[kt] = (f32x4){0.f, 0.f, 0.f, 0.f};
#pragma unroll
      for (int s = 0; s < 4; ++s) sacc[kt] = mfma16(kf[kt][s], qf[s], sacc[kt]);
    }
    PF(18, 3);
    const float scale = 0.08838834764831845f;
    float sv[8];
#pragma unroll
    for (int e = 0; e < 8; ++e) sv[e] = bf16_round(bf16_round(sacc[e >> 2][e & 3]) * scale);
    float mx = sv[0];
#pragma unroll
    for (int e = 1; e < 8; ++e) mx = fmaxf(mx, sv[e]);
    PF(21, 3);
    mx = fmaxf(mx, __shfl_xor(mx, 16));
    mx = fmaxf(mx, __shfl_xor(mx, 32));
    PF(24, 3);
    float sum = 0.f;
#pragma unroll
    for (int e = 0; e < 8; ++e) { sv[e] = __expf(sv[e] - mx); sum += sv[e]; }
    PF(27, 3);
    sum += __shfl_xor(sum, 16);
    sum += __shfl_xor(sum, 32);
    PF(30, 2);
    const float inv = 1.0f / sum;
    u32x4 pf;
#pragma unroll
    for (int p = 0; p < 4; ++p) pf[p] = pack2(sv[2 * p] * inv, sv[2 * p + 1] * inv);

    u32x4 vt[8];
    transpose8x8(vraw, vt);
    f32x4 oacc[8];
#pragma unroll
    for (int j = 0; j < 8; ++j) oacc[j] = mfma16(vt[j], pf, (f32x4){0.f, 0.f, 0.f, 0.f});

    const rsrc_t rO = make_rsrc(ws + WS_O, (unsigned)O_BYTES);
#pragma unroll
    for (int r = 0; r < 4; ++r) {
      u32x4 v;
#pragma unroll
      for (int p = 0; p < 4; ++p) v[p] = pack2(oacc[2 * p][r], oacc[2 * p + 1][r]);
      bstore_sc1(v, rO, (unsigned)(lane * 16), (unsigned)(((mt * 4 + wave) * 128 + h * 4 + r) * 1024));
    }
  }
  STAMP(0,4);
  asm volatile("s_waitcnt vmcnt(0)" ::: "memory");
  __syncthreads();
  if (wave == 0 && lane < 8) {
    const int cb = (mt * 4 + (lane & 3)) * 8 + (h >> 3) * 2 + (lane >> 2);
    __hip_atomic_store((u64*)(ws + WS_FLAG_O + cb * 128 + (h & 7) * 8), TAG, __ATOMIC_RELAXED, __HIP_MEMORY_SCOPE_AGENT);
  }
  STAMP(0,5);

#pragma unroll
  for (int t = 4; t < 8; ++t)
#pragma unroll
    for (int i = 0; i < 8; ++i)
      glds16<NT_WO>(wo_base, bo_off + (unsigned)((128 * ((t ^ rot) >> 2) + 8 * ((t ^ rot) & 3) + i) * DM * 2), abuf_l + ((t - 4) * 8 + i) * 1024);

  if (wave == 0) {
    u64* fl = (u64*)(ws + WS_FLAG_O + bid * 128) + (lane & 7);
    for (unsigned spin = 0; spin < SPIN_CAP; ++spin) {
      const u64 v = __hip_atomic_load(fl, __ATOMIC_RELAXED, __HIP_MEMORY_SCOPE_AGENT);
      if (__all(v == TAG)) break;
      __builtin_amdgcn_s_sleep(2);
    }
    if (lane < 8) __hip_atomic_store(fl, 0ull, __ATOMIC_RELAXED, __HIP_MEMORY_SCOPE_AGENT);
  }
  __syncthreads();
  __builtin_amdgcn_fence(__ATOMIC_ACQUIRE, "wavefront");
  STAMP(1,0);
#pragma unroll
  for (int m = 0; m < 4; ++m)
#pragma unroll
    for (int j = 0; j < 8; ++j) acc[m][j] = (f32x4){0.f, 0.f, 0.f, 0.f};
  {
    const rsrc_t rA = make_rsrc(ws + WS_O, (unsigned)O_BYTES);
    unsigned a_soff[4];
#pragma unroll
    for (int m = 0; m < 4; ++m) a_soff[m] = (unsigned)(((mt * 4 + ((wave + m) & 3)) * 128 + (k0 >> 5)) * 1024);
    constexpr int DA = 4, NSTEP = 8;
    u32x4 sa[DA][4];
    auto load_a = [&](int d, int t) {
#pragma unroll
      for (int m = 0; m < 4; ++m) sa[d][m] = bload_sc1(rA, (unsigned)(lane * 16), a_soff[m] + (t ^ rot) * 1024);
    };
#pragma unroll
    for (int t = 0; t < DA; ++t) load_a(t, t);
    u32x4 bt[2][8];
    transpose8x8(so[0], bt[0]);
    __builtin_amdgcn_sched_barrier(0);
#pragma unroll
    for (int t = 0; t < NSTEP; ++t) {
      u32x4 at[4];
#pragma unroll
      for (int m = 0; m < 4; ++m) at[m] = sa[t % DA][m];
      __builtin_amdgcn_sched_barrier(0);
      if (t == 0) STAMP(1,1);
      if (t + DA < NSTEP) load_a(t % DA, t + DA);
      if (t + 1 < 4) transpose8x8(so[t + 1], bt[(t + 1) & 1]);
      else if (t + 1 < NSTEP) {
        u32x4 raw[8];
#pragma unroll
        for (int i = 0; i < 8; ++i) raw[i] = *(const u32x4*)(abuf + ((t + 1 - 4) * 8 + i) * 1024 + lane * 16);
        transpose8x8(raw, bt[(t + 1) & 1]);
      }
      compute_stage(at, bt[t & 1], acc);
      INTERLEAVE_STEP();
      __builtin_amdgcn_sched_barrier(0);
    }
  }
  STAMP(1,2);
  f32x4 r[8];
  reduce_waves(lds, acc, r, wave, lane);
  STAMP(1,3);

  const int tile = mt * 8 + nt;
  const rsrc_t rs = make_rsrc(ws + WS_SLAB, (unsigned)SLAB_BYTES);
  const unsigned sl_off = (unsigned)((tile * 16 + wave) * 8192 + lane * 16);
#pragma unroll
  for (int j = 0; j < 8; ++j) bstore_sc1(__builtin_bit_cast(u32x4, r[j]), rs, sl_off, (unsigned)(ks * 32768 + j * 1024));
  asm volatile("s_waitcnt vmcnt(0)" ::: "memory");
  __syncthreads();
  if (wave == 0 && lane < 4) {
    const int ob = (mt * 4 + (nt & 3)) * 8 + lane * 2 + (nt >> 2);
    __hip_atomic_store((u64*)(ws + WS_FLAG_S + ob * 128 + ks * 8), TAG, __ATOMIC_RELAXED, __HIP_MEMORY_SCOPE_AGENT);
  }
  STAMP(1,4);
  if (wave != ks) { RSTAMP(1,7); return; }
  {
    u64* fl = (u64*)(ws + WS_FLAG_S + bid * 128) + (lane & 3);
    for (unsigned spin = 0; spin < SPIN_CAP; ++spin) {
      const u64 v = __hip_atomic_load(fl, __ATOMIC_RELAXED, __HIP_MEMORY_SCOPE_AGENT);
      if (__all(v == TAG)) break;
      __builtin_amdgcn_s_sleep(1);
    }
    if (lane < 4) __hip_atomic_store(fl, 0ull, __ATOMIC_RELAXED, __HIP_MEMORY_SCOPE_AGENT);
  }
  __builtin_amdgcn_fence(__ATOMIC_ACQUIRE, "wavefront");
  f32x4 s0[8], s1[8], s2[8], s3[8];
#pragma unroll
  for (int j = 0; j < 8; ++j) {
    s0[j] = __builtin_bit_cast(f32x4, bload_sc1(rs, sl_off, (unsigned)(0 * 32768 + j * 1024)));
    s1[j] = __builtin_bit_cast(f32x4, bload_sc1(rs, sl_off, (unsigned)(1 * 32768 + j * 1024)));
    s2[j] = __builtin_bit_cast(f32x4, bload_sc1(rs, sl_off, (unsigned)(2 * 32768 + j * 1024)));
    s3[j] = __builtin_bit_cast(f32x4, bload_sc1(rs, sl_off, (unsigned)(3 * 32768 + j * 1024)));
  }
#pragma unroll
  for (int j = 0; j < 8; ++j) r[j] = (s0[j] + s1[j]) + (s2[j] + s3[j]);
  char* obase = (char*)out + (size_t)(mt * 64 + wave * 16 + 4 * g) * DM * 2 + nt * 256 + c * 16;
#pragma unroll
  for (int rr = 0; rr < 4; ++rr) {
    u32x4 v;
#pragma unroll
    for (int p = 0; p < 4; ++p) v[p] = pack2(r[2 * p][rr], r[2 * p + 1][rr]);
    *(u32x4*)(obase + (size_t)rr * DM * 2) = v;
  }
  DRAIN_STAMP(); STAMP(1,5); RSTAMP(1,7);
}

extern "C" void kernel_launch(void* const* d_in, const int* in_sizes, int n_in,
                              void* d_out, int out_size, void* d_ws, size_t ws_size,
                              hipStream_t stream) {
  const __hip_bfloat16* x  = (const __hip_bfloat16*)d_in[0];
  const __hip_bfloat16* kc = (const __hip_bfloat16*)d_in[1];
  const __hip_bfloat16* vc = (const __hip_bfloat16*)d_in[2];
  const __hip_bfloat16* wq = (const __hip_bfloat16*)d_in[3];
  const __hip_bfloat16* wo = (const __hip_bfloat16*)d_in[4];
  __hip_bfloat16* out = (__hip_bfloat16*)d_out;
  char* ws = (char*)d_ws;
  (void)hipFuncSetAttribute((const void*)k_fused, hipFuncAttributeMaxDynamicSharedMemorySize, LDS_BYTES);
  k_fused<<<256, 256, LDS_BYTES, stream>>>(x, kc, vc, wq, wo, ws, out);
}
```

```cpp
#define NT_WQ 0
#define NT_X 0
#define NT_KV 0
#define NT_WO 0
#include <hip/hip_runtime.h>
#include <hip/hip_bf16.h>
#include <stdint.h>

typedef short bf16x8 __attribute__((ext_vector_type(8)));
typedef float f32x4 __attribute__((ext_vector_type(4)));
typedef unsigned u32x4 __attribute__((ext_vector_type(4)));
typedef unsigned long long u64;

#define DI __device__ __forceinline__
#define STAMP(K,i)
#define RSTAMP(K,i)
#define DRAIN_STAMP()

constexpr int NB = 16, SQ = 32, DM = 1024, NH = 32, SK = 32, DH = 128;
constexpr int MROWS = NB * SQ;
constexpr int HD = NH * DH;

constexpr size_t WS_FLAG_O = 0;
constexpr size_t WS_FLAG_S = 256 * 128;
constexpr size_t WS_O = 2 * 256 * 128;
constexpr size_t O_BYTES = (size_t)MROWS * HD * 2;
constexpr size_t WS_SLAB = WS_O + O_BYTES;
constexpr size_t SLAB_BYTES = (size_t)64 * 4 * 32768;
constexpr u64 TAG = 0x9E3779B97F4A7C15ull;
constexpr unsigned SPIN_CAP = 1u << 20;

DI f32x4 mfma16(u32x4 a, u32x4 b, f32x4 c) {
  return __builtin_amdgcn_mfma_f32_16x16x32_bf16(__builtin_bit_cast(bf16x8, a), __builtin_bit_cast(bf16x8, b), c, 0, 0, 0);
}
typedef __bf16 bf16x2_t __attribute__((ext_vector_type(2)));
typedef float f32x2 __attribute__((ext_vector_type(2)));
DI unsigned pack2(float lo, float hi) {
  f32x2 f = {lo, hi};
  return __builtin_bit_cast(unsigned, __builtin_convertvector(f, bf16x2_t));
}
DI float bf16_round(float f) { return __uint_as_float(pack2(f, 0.f) << 16); }

DI void transpose8x8(const u32x4 (&raw)[8], u32x4 (&out)[8]) {
#pragma unroll
  for (int d = 0; d < 4; ++d) {
#pragma unroll
    for (int p = 0; p < 4; ++p) {
      out[2 * d][p]     = __builtin_amdgcn_perm(raw[2 * p + 1][d], raw[2 * p][d], 0x05040100u);
      out[2 * d + 1][p] = __builtin_amdgcn_perm(raw[2 * p + 1][d], raw[2 * p][d], 0x07060302u);
    }
  }
}

template <int NT>
DI void glds16(const char* sbase, unsigned voff, unsigned lds_dst) {
  unsigned keep;
  if (NT)
    asm volatile("s_mov_b32 %0, m0\n\ts_mov_b32 m0, %3\n\ts_nop 0\n\tglobal_load_lds_dwordx4 %1, %2 nt\n\ts_mov_b32 m0, %0"
                 : "=&s"(keep) : "v"(voff), "s"(sbase), "s"(lds_dst) : "memory");
  else
    asm volatile("s_mov_b32 %0, m0\n\ts_mov_b32 m0, %3\n\ts_nop 0\n\tglobal_load_lds_dwordx4 %1, %2\n\ts_mov_b32 m0, %0"
                 : "=&s"(keep) : "v"(voff), "s"(sbase), "s"(lds_dst) : "memory");
}

typedef __amdgpu_buffer_rsrc_t rsrc_t;
DI rsrc_t make_rsrc(const void* p, unsigned bytes) { return __builtin_amdgcn_make_buffer_rsrc((void*)p, 0, (int)bytes, 0x00020000); }
template <int NT> DI u32x4 bload(rsrc_t rs, unsigned voff, unsigned soff) { return __builtin_amdgcn_raw_buffer_load_b128(rs, (int)voff, (int)soff, NT ? 2 : 0); }
DI u32x4 bload_sc1(rsrc_t rs, unsigned voff, unsigned soff) { return __builtin_amdgcn_raw_buffer_load_b128(rs, (int)voff, (int)soff, 16); }
DI void bstore_sc1(u32x4 v, rsrc_t rs, unsigned voff, unsigned soff) { __builtin_amdgcn_raw_buffer_store_b128(v, rs, (int)voff, (int)soff, 16); }

DI void compute_stage(const u32x4 (&sa)[4], const u32x4 (&bt)[8], f32x4 (&acc)[4][8]) {
#pragma unroll
  for (int j = 0; j < 8; ++j)
#pragma unroll
    for (int m = 0; m < 4; ++m) acc[m][j] = mfma16(sa[m], bt[j], acc[m][j]);
}

DI void reduce_waves(char* lds, const f32x4 (&acc)[4][8], f32x4 (&res)[8], int wave, int lane) {
#pragma unroll
  for (int m = 1; m < 4; ++m)
#pragma unroll
    for (int j = 0; j < 8; ++j) *(f32x4*)(lds + wave * 32768 + ((m - 1) * 8 + j) * 1024 + lane * 16) = acc[m][j];
  __syncthreads();
#pragma unroll
  for (int j = 0; j < 8; ++j) res[j] = acc[0][j];
#pragma unroll
  for (int m = 1; m < 4; ++m) {
    const int w = (wave - m) & 3;
#pragma unroll
    for (int j = 0; j < 8; ++j) res[j] += *(const f32x4*)(lds + w * 32768 + ((m - 1) * 8 + j) * 1024 + lane * 16);
  }
}

#define INTERLEAVE_STEP()                                                                   \
  _Pragma("unroll") for (int i_ = 0; i_ < 32; ++i_) {                                       \
    __builtin_amdgcn_sched_group_barrier(0x008, 1, 0);                                      \
    if (i_ < 24 && (i_ & 1)) __builtin_amdgcn_sched_group_barrier(0x020, 1, 0);             \
    if (i_ >= 8 && i_ < 20) __builtin_amdgcn_sched_group_barrier(0x100, 1, 0);              \
    if (i_ >= 16) __builtin_amdgcn_sched_group_barrier(0x002, 2, 0);                        \
  }

constexpr int QROW = 272;
constexpr int LDS_BYTES = 5 * 32768;

__global__ __launch_bounds__(256, 1) void k_fused(const __hip_bfloat16* __restrict__ x,
                                                  const __hip_bfloat16* __restrict__ kc,
                                                  const __hip_bfloat16* __restrict__ vc,
                                                  const __hip_bfloat16* __restrict__ wq,
                                                  const __hip_bfloat16* __restrict__ wo,
                                                  char* __restrict__ ws,
                                                  __hip_bfloat16* __restrict__ out) {
  extern __shared__ __attribute__((aligned(16))) char lds[];
  const int tid = threadIdx.x, lane = tid & 63;
  const int wave = __builtin_amdgcn_readfirstlane(tid >> 6);
  const int c = lane & 15, g = lane >> 4;
  const int bid = blockIdx.x;
  const int xg = bid & 7, idx = bid >> 3;
  const int mt = idx >> 2;
  const int h = xg + 8 * (idx & 3);
  const int ks = xg >> 1, nt = (xg & 1) * 4 + (idx & 3);
  RSTAMP(0,6); STAMP(0,0);

  char* abuf = lds + wave * 32768;
  const unsigned abuf_l = (unsigned)(uintptr_t)(__attribute__((address_space(3))) char*)abuf;
  f32x4 acc[4][8];
#pragma unroll
  for (int m = 0; m < 4; ++m)
#pragma unroll
    for (int j = 0; j < 8; ++j) acc[m][j] = (f32x4){0.f, 0.f, 0.f, 0.f};

  const char* xrow = (const char*)x + (size_t)(mt * 64) * (DM * 2) + wave * 512;
  auto dma_x = [&](int hh, int i) {
    const int row = 4 * i + g;
    const unsigned off = (unsigned)(row * (DM * 2) + hh * 256 + ((c ^ (row & 15)) << 4));
    glds16<NT_X>(xrow, off, abuf_l + hh * 16384 + i * 1024);
  };
  const rsrc_t rq = make_rsrc((const char*)wq + (size_t)(wave * 256) * HD * 2 + h * DH * 2, 256 * HD * 2);
  const unsigned bq_off = (unsigned)(g * 8 * HD * 2 + c * 16);
  auto load_wq = [&](u32x4 (&sb)[8], int step) {
#pragma unroll
    for (int i = 0; i < 8; ++i) sb[i] = bload<NT_WQ>(rq, bq_off, (unsigned)((step * 32 + i) * HD * 2));
  };
  const int b = mt * 2 + (wave >> 1);
  const char* kbase = (const char*)kc + (size_t)(b * NH + h) * SK * DH * 2;
  const char* vbase = (const char*)vc + (size_t)(b * NH + h) * SK * DH * 2;
  char* kvbuf = lds + 4 * 32768 + (wave >> 1) * 16384;
  auto dma_kv = [&]() {
    const unsigned kv_l = (unsigned)(uintptr_t)(__attribute__((address_space(3))) char*)kvbuf;
    if (wave & 1) {
#pragma unroll
      for (int e = 0; e < 8; ++e) glds16<NT_KV>(vbase, (unsigned)((16 * (e >> 2) + 4 * g + (e & 3)) * 256 + c * 16), kv_l + 8192 + e * 1024);
    } else {
#pragma unroll
      for (int e = 0; e < 8; ++e) glds16<NT_KV>(kbase, (unsigned)(((e >> 2) * 16 + c) * 256 + (e & 3) * 64 + g * 16), kv_l + e * 1024);
    }
  };

  const int k0 = ks * 1024 + wave * 256;
  const char* wo_base = (const char*)wo + (size_t)k0 * DM * 2 + nt * 256;
  const rsrc_t ro = make_rsrc(wo_base, 256 * DM * 2);
  const unsigned bo_off = (unsigned)(g * 32 * DM * 2 + c * 16);
  u32x4 so[4][8];
  const int rot = mt;

  {
    constexpr int DEPTH = 4, NSTEP = 8;
    u32x4 sb[DEPTH][8];
#pragma unroll
    for (int i = 0; i < 16; ++i) dma_x(rot >> 2, i);
    load_wq(sb[0], 0 ^ rot);
    load_wq(sb[1], 1 ^ rot);
    load_wq(sb[2], 2 ^ rot);
    __builtin_amdgcn_sched_barrier(0);
    unsigned aoff[4];
#pragma unroll
    for (int s = 0; s < 4; ++s) aoff[s] = (unsigned)(c * 256 + (((4 * (s ^ (rot & 3)) + g) ^ c) << 4));
    auto read_a = [&](u32x4 (&at)[4], int s) {
#pragma unroll
      for (int m = 0; m < 4; ++m)
        at[m] = *(const u32x4*)(abuf + ((wave + m) & 3) * 4096 + ((s >> 2) ^ (rot >> 2)) * 16384 + aoff[s & 3]);
    };
    u32x4 at[2][4], bt[2][8];
    asm volatile("s_waitcnt vmcnt(24)" ::: "memory");
    read_a(at[0], 0);
    transpose8x8(sb[0], bt[0]);
    __builtin_amdgcn_sched_barrier(0);
    STAMP(0,1);
#pragma unroll
    for (int s = 0; s < NSTEP; ++s) {
      if (s < 3) {
#pragma unroll
        for (int i = 0; i < (s == 2 ? 6 : 5); ++i) dma_x(1 ^ (rot >> 2), s * 5 + i);
      }
      if (s + 3 < NSTEP) load_wq(sb[(s + 3) % DEPTH], (s + 3) ^ rot);
      if (s == 4) dma_kv();
      if (s >= 6) {
#pragma unroll
        for (int u = (s - 6) * 8; u < (s - 5) * 8; ++u) {
          const int ta = (u >> 3) ^ rot;
          so[u >> 3][u & 7] = bload<NT_WO>(ro, bo_off, (unsigned)((128 * (ta >> 2) + 8 * (ta & 3) + (u & 7)) * DM * 2));
        }
      }
      if (s == 3) asm volatile("s_waitcnt vmcnt(16)" ::: "memory");
      if (s + 1 < NSTEP) { read_a(at[(s + 1) & 1], s + 1); transpose8x8(sb[(s + 1) % DEPTH], bt[(s + 1) & 1]); }
      compute_stage(at[s & 1], bt[s & 1], acc);
      INTERLEAVE_STEP();
      __builtin_amdgcn_sched_barrier(0);
    }
  }
  STAMP(0,2);
  asm volatile("s_waitcnt vmcnt(16)" ::: "memory");
#define PF(a, n) do { __builtin_amdgcn_sched_barrier(0); _Pragma("unroll") for (int u_ = 16 + (a) / 2; u_ < 16 + ((a) + (n)) / 2; ++u_) { \
    const int ta_ = (u_ >> 3) ^ rot; \
    so[u_ >> 3][u_ & 7] = bload<NT_WO>(ro, bo_off, (unsigned)((128 * (ta_ >> 2) + 8 * (ta_ & 3) + (u_ & 7)) * DM * 2)); } \
    __builtin_amdgcn_sched_barrier(0); } while (0)
  {
    f32x4 q[8];
    PF(0, 3);
#pragma unroll
    for (int m = 1; m < 4; ++m) {
#pragma unroll
      for (int j = 0; j < 8; ++j) *(f32x4*)(lds + wave * 32768 + ((m - 1) * 8 + j) * 1024 + lane * 16) = acc[m][j];
      if (m == 1) PF(3, 2); else if (m == 2) PF(5, 2); else PF(7, 2);
    }
    __syncthreads();
#pragma unroll
    for (int j = 0; j < 8; ++j) q[j] = acc[0][j];
#pragma unroll
    for (int m = 1; m < 4; ++m) {
      const int w = (wave - m) & 3;
#pragma unroll
      for (int j = 0; j < 8; ++j) q[j] += *(const f32x4*)(lds + w * 32768 + ((m - 1) * 8 + j) * 1024 + lane * 16);
      if (m == 1) PF(9, 2); else if (m == 2) PF(11, 2); else PF(13, 2);
    }
    STAMP(0,3);
    char* qimg = abuf + 24 * 1024;
#pragma unroll
    for (int r = 0; r < 4; ++r) {
      u32x4 v;
#pragma unroll
      for (int p = 0; p < 4; ++p) v[p] = pack2(q[2 * p][r], q[2 * p + 1][r]);
      *(u32x4*)(qimg + (4 * g + r) * QROW + c * 16) = v;
    }
    __builtin_amdgcn_wave_barrier();
    PF(15, 3);
    u32x4 qf[4];
#pragma unroll
    for (int s = 0; s < 4; ++s) qf[s] = *(const u32x4*)(qimg + c * QROW + s * 64 + g * 16);
    u32x4 kf[2][4], vraw[8];
#pragma unroll
    for (int e = 0; e < 8; ++e) kf[e >> 2][e & 3] = *(const u32x4*)(kvbuf + e * 1024 + lane * 16);
#pragma unroll
    for (int e = 0; e < 8; ++e) vraw[e] = *(const u32x4*)(kvbuf + 8192 + e * 1024 + lane * 16);

    f32x4 sacc[2];
#pragma unroll
    for (int kt = 0; kt < 2; ++kt) {
      sacc[kt] = (f32x4){0.f, 0.f, 0.f, 0.f};
#pragma unroll
      for (int s = 0; s < 4; ++s) sacc[kt] = mfma16(kf[kt][s], qf[s], sacc[kt]);
    }
    PF(18, 3);
    const float scale = 0.08838834764831845f;
    float sv[8];
#pragma unroll
    for (int e = 0; e < 8; ++e) sv[e] = bf16_round(bf16_round(sacc[e >> 2][e & 3]) * scale);
    float mx = sv[0];
#pragma unroll
    for (int e = 1; e < 8; ++e) mx = fmaxf(mx, sv[e]);
    PF(21, 3);
    mx = fmaxf(mx, __shfl_xor(mx, 16));
    mx = fmaxf(mx, __shfl_xor(mx, 32));
    PF(24, 3);
    float sum = 0.f;
#pragma unroll
    for (int e = 0; e < 8; ++e) { sv[e] = __expf(sv[e] - mx); sum += sv[e]; }
    PF(27, 3);
    sum += __shfl_xor(sum, 16);
    sum += __shfl_xor(sum, 32);
    PF(30, 2);
    const float inv = 1.0f / sum;
    u32x4 pf;
#pragma unroll
    for (int p = 0; p < 4; ++p) pf[p] = pack2(sv[2 * p] * inv, sv[2 * p + 1] * inv);

    u32x4 vt[8];
    transpose8x8(vraw, vt);
    f32x4 oacc[8];
#pragma unroll
    for (int j = 0; j < 8; ++j) oacc[j] = mfma16(vt[j], pf, (f32x4){0.f, 0.f, 0.f, 0.f});

    const rsrc_t rO = make_rsrc(ws + WS_O, (unsigned)O_BYTES);
#pragma unroll
    for (int r = 0; r < 4; ++r) {
      u32x4 v;
#pragma unroll
      for (int p = 0; p < 4; ++p) v[p] = pack2(oacc[2 * p][r], oacc[2 * p + 1][r]);
      bstore_sc1(v, rO, (unsigned)(lane * 16), (unsigned)(((mt * 4 + wave) * 128 + h * 4 + r) * 1024));
    }
  }
  STAMP(0,4);
  asm volatile("s_waitcnt vmcnt(0)" ::: "memory");
  __syncthreads();
  if (wave == 0 && lane < 8) {
    const int cb = (mt * 4 + (lane & 3)) * 8 + (h >> 3) * 2 + (lane >> 2);
    __hip_atomic_store((u64*)(ws + WS_FLAG_O + cb * 128 + (h & 7) * 8), TAG, __ATOMIC_RELAXED, __HIP_MEMORY_SCOPE_AGENT);
  }
  STAMP(0,5);

#pragma unroll
  for (int t = 4; t < 8; ++t)
#pragma unroll
    for (int i = 0; i < 8; ++i)
      glds16<NT_WO>(wo_base, bo_off + (unsigned)((128 * ((t ^ rot) >> 2) + 8 * ((t ^ rot) & 3) + i) * DM * 2), abuf_l + ((t - 4) * 8 + i) * 1024);

  if (wave == 0) {
    u64* fl = (u64*)(ws + WS_FLAG_O + bid * 128) + (lane & 7);
    for (unsigned spin = 0; spin < SPIN_CAP; ++spin) {
      const u64 v = __hip_atomic_load(fl, __ATOMIC_RELAXED, __HIP_MEMORY_SCOPE_AGENT);
      if (__all(v == TAG)) break;
      __builtin_amdgcn_s_sleep(2);
    }
    if (lane < 8) __hip_atomic_store(fl, 0ull, __ATOMIC_RELAXED, __HIP_MEMORY_SCOPE_AGENT);
  }
  __syncthreads();
  __builtin_amdgcn_fence(__ATOMIC_ACQUIRE, "wavefront");
  STAMP(1,0);
#pragma unroll
  for (int m = 0; m < 4; ++m)
#pragma unroll
    for (int j = 0; j < 8; ++j) acc[m][j] = (f32x4){0.f, 0.f, 0.f, 0.f};
  {
    const rsrc_t rA = make_rsrc(ws + WS_O, (unsigned)O_BYTES);
    unsigned a_soff[4];
#pragma unroll
    for (int m = 0; m < 4; ++m) a_soff[m] = (unsigned)(((mt * 4 + ((wave + m) & 3)) * 128 + (k0 >> 5)) * 1024);
    constexpr int DA = 4, NSTEP = 8;
    u32x4 sa[DA][4];
    auto load_a = [&](int d, int t) {
#pragma unroll
      for (int m = 0; m < 4; ++m) sa[d][m] = bload_sc1(rA, (unsigned)(lane * 16), a_soff[m] + (t ^ rot) * 1024);
    };
#pragma unroll
    for (int t = 0; t < DA; ++t) load_a(t, t);
    u32x4 bt[2][8];
    transpose8x8(so[0], bt[0]);
    __builtin_amdgcn_sched_barrier(0);
#pragma unroll
    for (int t = 0; t < NSTEP; ++t) {
      u32x4 at[4];
#pragma unroll
      for (int m = 0; m < 4; ++m) at[m] = sa[t % DA][m];
      __builtin_amdgcn_sched_barrier(0);
      if (t == 0) STAMP(1,1);
      if (t + DA < NSTEP) load_a(t % DA, t + DA);
      if (t + 1 < 4) transpose8x8(so[t + 1], bt[(t + 1) & 1]);
      else if (t + 1 < NSTEP) {
        u32x4 raw[8];
#pragma unroll
        for (int i = 0; i < 8; ++i) raw[i] = *(const u32x4*)(abuf + ((t + 1 - 4) * 8 + i) * 1024 + lane * 16);
        transpose8x8(raw, bt[(t + 1) & 1]);
      }
      compute_stage(at, bt[t & 1], acc);
      INTERLEAVE_STEP();
      __builtin_amdgcn_sched_barrier(0);
    }
  }
  STAMP(1,2);
  f32x4 r[8];
  reduce_waves(lds, acc, r, wave, lane);
  STAMP(1,3);

  const int tile = mt * 8 + nt;
  const rsrc_t rs = make_rsrc(ws + WS_SLAB, (unsigned)SLAB_BYTES);
  const unsigned sl_off = (unsigned)((tile * 16 + wave) * 8192 + lane * 16);
#pragma unroll
  for (int j = 0; j < 8; ++j) bstore_sc1(__builtin_bit_cast(u32x4, r[j]), rs, sl_off, (unsigned)(ks * 32768 + j * 1024));
  asm volatile("s_waitcnt vmcnt(0)" ::: "memory");
  __syncthreads();
  if (wave == 0 && lane < 4) {
    const int ob = (mt * 4 + (nt & 3)) * 8 + lane * 2 + (nt >> 2);
    __hip_atomic_store((u64*)(ws + WS_FLAG_S + ob * 128 + ks * 8), TAG, __ATOMIC_RELAXED, __HIP_MEMORY_SCOPE_AGENT);
  }
  STAMP(1,4);
  if (wave != ks) { RSTAMP(1,7); return; }
  {
    u64* fl = (u64*)(ws + WS_FLAG_S + bid * 128) + (lane & 3);
    for (unsigned spin = 0; spin < SPIN_CAP; ++spin) {
      const u64 v = __hip_atomic_load(fl, __ATOMIC_RELAXED, __HIP_MEMORY_SCOPE_AGENT);
      if (__all(v == TAG)) break;
      __builtin_amdgcn_s_sleep(1);
    }
    if (lane < 4) __hip_atomic_store(fl, 0ull, __ATOMIC_RELAXED, __HIP_MEMORY_SCOPE_AGENT);
  }
  __builtin_amdgcn_fence(__ATOMIC_ACQUIRE, "wavefront");
  f32x4 s0[8], s1[8], s2[8], s3[8];
#pragma unroll
  for (int j = 0; j < 8; ++j) {
    s0[j] = __builtin_bit_cast(f32x4, bload_sc1(rs, sl_off, (unsigned)(0 * 32768 + j * 1024)));
    s1[j] = __builtin_bit_cast(f32x4, bload_sc1(rs, sl_off, (unsigned)(1 * 32768 + j * 1024)));
    s2[j] = __builtin_bit_cast(f32x4, bload_sc1(rs, sl_off, (unsigned)(2 * 32768 + j * 1024)));
    s3[j] = __builtin_bit_cast(f32x4, bload_sc1(rs, sl_off, (unsigned)(3 * 32768 + j * 1024)));
  }
#pragma unroll
  for (int j = 0; j < 8; ++j) r[j] = (s0[j] + s1[j]) + (s2[j] + s3[j]);
  char* obase = (char*)out + (size_t)(mt * 64 + wave * 16 + 4 * g) * DM * 2 + nt * 256 + c * 16;
#pragma unroll
  for (int rr = 0; rr < 4; ++rr) {
    u32x4 v;
#pragma unroll
    for (int p = 0; p < 4; ++p) v[p] = pack2(r[2 * p][rr], r[2 * p + 1][rr]);
    *(u32x4*)(obase + (size_t)rr * DM * 2) = v;
  }
  DRAIN_STAMP(); STAMP(1,5); RSTAMP(1,7);
}

extern "C" void kernel_launch(void* const* d_in, const int* in_sizes, int n_in,
                              void* d_out, int out_size, void* d_ws, size_t ws_size,
                              hipStream_t stream) {
  const __hip_bfloat16* x  = (const __hip_bfloat16*)d_in[0];
  const __hip_bfloat16* kc = (const __hip_bfloat16*)d_in[1];
  const __hip_bfloat16* vc = (const __hip_bfloat16*)d_in[2];
  const __hip_bfloat16* wq = (const __hip_bfloat16*)d_in[3];
  const __hip_bfloat16* wo = (const __hip_bfloat16*)d_in[4];
  __hip_bfloat16* out = (__hip_bfloat16*)d_out;
  char* ws = (char*)d_ws;
  (void)hipFuncSetAttribute((const void*)k_fused, hipFuncAttributeMaxDynamicSharedMemorySize, LDS_BYTES);
  k_fused<<<256, 256, LDS_BYTES, stream>>>(x, kc, vc, wq, wo, ws, out);
}
```

```cpp
#define NT_WQ 0
#define NT_X 0
#define NT_KV 0
#define NT_WO 0
#include <hip/hip_runtime.h>
#include <hip/hip_bf16.h>
#include <stdint.h>

typedef short bf16x8 __attribute__((ext_vector_type(8)));
typedef float f32x4 __attribute__((ext_vector_type(4)));
typedef unsigned u32x4 __attribute__((ext_vector_type(4)));
typedef unsigned long long u64;

#define DI __device__ __forceinline__
#define STAMP(K,i)
#define RSTAMP(K,i)
#define DRAIN_STAMP()

constexpr int NB = 16, SQ = 32, DM = 1024, NH = 32, SK = 32, DH = 128;
constexpr int MROWS = NB * SQ;
constexpr int HD = NH * DH;

constexpr size_t WS_FLAG_O = 0;
constexpr size_t WS_FLAG_S = 256 * 128;
constexpr size_t WS_O = 2 * 256 * 128;
constexpr size_t O_BYTES = (size_t)MROWS * HD * 2;
constexpr size_t WS_SLAB = WS_O + O_BYTES;
constexpr size_t SLAB_BYTES = (size_t)64 * 4 * 32768;
constexpr u64 TAG = 0x9E3779B97F4A7C15ull;
constexpr unsigned SPIN_CAP = 1u << 20;

DI f32x4 mfma16(u32x4 a, u32x4 b, f32x4 c) {
  return __builtin_amdgcn_mfma_f32_16x16x32_bf16(__builtin_bit_cast(bf16x8, a), __builtin_bit_cast(bf16x8, b), c, 0, 0, 0);
}
typedef __bf16 bf16x2_t __attribute__((ext_vector_type(2)));
typedef float f32x2 __attribute__((ext_vector_type(2)));
DI unsigned pack2(float lo, float hi) {
  f32x2 f = {lo, hi};
  return __builtin_bit_cast(unsigned, __builtin_convertvector(f, bf16x2_t));
}
DI float bf16_round(float f) { return __uint_as_float(pack2(f, 0.f) << 16); }

DI void transpose8x8(const u32x4 (&raw)[8], u32x4 (&out)[8]) {
#pragma unroll
  for (int d = 0; d < 4; ++d) {
#pragma unroll
    for (int p = 0; p < 4; ++p) {
      out[2 * d][p]     = __builtin_amdgcn_perm(raw[2 * p + 1][d], raw[2 * p][d], 0x05040100u);
      out[2 * d + 1][p] = __builtin_amdgcn_perm(raw[2 * p + 1][d], raw[2 * p][d], 0x07060302u);
    }
  }
}

template <int NT>
DI void glds16(const char* sbase, unsigned voff, unsigned lds_dst) {
  unsigned keep;
  if (NT)
    asm volatile("s_mov_b32 %0, m0\n\ts_mov_b32 m0, %3\n\ts_nop 0\n\tglobal_load_lds_dwordx4 %1, %2 nt\n\ts_mov_b32 m0, %0"
                 : "=&s"(keep) : "v"(voff), "s"(sbase), "s"(lds_dst) : "memory");
  else
    asm volatile("s_mov_b32 %0, m0\n\ts_mov_b32 m0, %3\n\ts_nop 0\n\tglobal_load_lds_dwordx4 %1, %2\n\ts_mov_b32 m0, %0"
                 : "=&s"(keep) : "v"(voff), "s"(sbase), "s"(lds_dst) : "memory");
}

typedef __amdgpu_buffer_rsrc_t rsrc_t;
DI rsrc_t make_rsrc(const void* p, unsigned bytes) { return __builtin_amdgcn_make_buffer_rsrc((void*)p, 0, (int)bytes, 0x00020000); }
template <int NT> DI u32x4 bload(rsrc_t rs, unsigned voff, unsigned soff) { return __builtin_amdgcn_raw_buffer_load_b128(rs, (int)voff, (int)soff, NT ? 2 : 0); }
DI u32x4 bload_sc1(rsrc_t rs, unsigned voff, unsigned soff) { return __builtin_amdgcn_raw_buffer_load_b128(rs, (int)voff, (int)soff, 16); }
DI void bstore_sc1(u32x4 v, rsrc_t rs, unsigned voff, unsigned soff) { __builtin_amdgcn_raw_buffer_store_b128(v, rs, (int)voff, (int)soff, 16); }

DI void compute_stage(const u32x4 (&sa)[4], const u32x4 (&bt)[8], f32x4 (&acc)[4][8]) {
#pragma unroll
  for (int j = 0; j < 8; ++j)
#pragma unroll
    for (int m = 0; m < 4; ++m) acc[m][j] = mfma16(sa[m], bt[j], acc[m][j]);
}

DI void reduce_waves(char* lds, const f32x4 (&acc)[4][8], f32x4 (&res)[8], int wave, int lane) {
#pragma unroll
  for (int m = 1; m < 4; ++m)
#pragma unroll
    for (int j = 0; j < 8; ++j) *(f32x4*)(lds + wave * 32768 + ((m - 1) * 8 + j) * 1024 + lane * 16) = acc[m][j];
  __syncthreads();
#pragma unroll
  for (int j = 0; j < 8; ++j) res[j] = acc[0][j];
#pragma unroll
  for (int m = 1; m < 4; ++m) {
    const int w = (wave - m) & 3;
#pragma unroll
    for (int j = 0; j < 8; ++j) res[j] += *(const f32x4*)(lds + w * 32768 + ((m - 1) * 8 + j) * 1024 + lane * 16);
  }
}

#define INTERLEAVE_STEP()                                                                   \
  _Pragma("unroll") for (int i_ = 0; i_ < 32; ++i_) {                                       \
    __builtin_amdgcn_sched_group_barrier(0x008, 1, 0);                                      \
    if (i_ < 24 && (i_ & 1)) __builtin_amdgcn_sched_group_barrier(0x020, 1, 0);             \
    if (i_ >= 8 && i_ < 20) __builtin_amdgcn_sched_group_barrier(0x100, 1, 0);              \
    if (i_ >= 16) __builtin_amdgcn_sched_group_barrier(0x002, 2, 0);                        \
  }

constexpr int QROW = 272;
constexpr int LDS_BYTES = 5 * 32768;

__global__ __launch_bounds__(256, 1) void k_fused(const __hip_bfloat16* __restrict__ x,
                                                  const __hip_bfloat16* __restrict__ kc,
                                                  const __hip_bfloat16* __restrict__ vc,
                                                  const __hip_bfloat16* __restrict__ wq,
                                                  const __hip_bfloat16* __restrict__ wo,
                                                  char* __restrict__ ws,
                                                  __hip_bfloat16* __restrict__ out) {
  extern __shared__ __attribute__((aligned(16))) char lds[];
  const int tid = threadIdx.x, lane = tid & 63;
  const int wave = __builtin_amdgcn_readfirstlane(tid >> 6);
  const int c = lane & 15, g = lane >> 4;
  const int bid = blockIdx.x;
  const int xg = bid & 7, idx = bid >> 3;
  const int mt = idx >> 2;
  const int h = xg + 8 * (idx & 3);
  const int ks = xg >> 1, nt = (xg & 1) * 4 + (idx & 3);
  RSTAMP(0,6); STAMP(0,0);

  char* abuf = lds + wave * 32768;
  const unsigned abuf_l = (unsigned)(uintptr_t)(__attribute__((address_space(3))) char*)abuf;
  f32x4 acc[4][8];
#pragma unroll
  for (int m = 0; m < 4; ++m)
#pragma unroll
    for (int j = 0; j < 8; ++j) acc[m][j] = (f32x4){0.f, 0.f, 0.f, 0.f};

  const char* xrow = (const char*)x + (size_t)(mt * 64) * (DM * 2) + wave * 512;
  auto dma_x = [&](int hh, int i) {
    const int row = 4 * i + g;
    const unsigned off = (unsigned)(row * (DM * 2) + hh * 256 + ((c ^ (row & 15)) << 4));
    glds16<NT_X>(xrow, off, abuf_l + hh * 16384 + i * 1024);
  };
  const rsrc_t rq = make_rsrc((const char*)wq + (size_t)(wave * 256) * HD * 2 + h * DH * 2, 256 * HD * 2);
  const unsigned bq_off = (unsigned)(g * 8 * HD * 2 + c * 16);
  auto load_wq = [&](u32x4 (&sb)[8], int step) {
#pragma unroll
    for (int i = 0; i < 8; ++i) sb[i] = bload<NT_WQ>(rq, bq_off, (unsigned)((step * 32 + i) * HD * 2));
  };
  const int b = mt * 2 + (wave >> 1);
  const char* kbase = (const char*)kc + (size_t)(b * NH + h) * SK * DH * 2;
  const char* vbase = (const char*)vc + (size_t)(b * NH + h) * SK * DH * 2;
  char* kvbuf = lds + 4 * 32768 + (wave >> 1) * 16384;
  auto dma_kv = [&]() {
    const unsigned kv_l = (unsigned)(uintptr_t)(__attribute__((address_space(3))) char*)kvbuf;
    if (wave & 1) {
#pragma unroll
      for (int e = 0; e < 8; ++e) glds16<NT_KV>(vbase, (unsigned)((16 * (e >> 2) + 4 * g + (e & 3)) * 256 + c * 16), kv_l + 8192 + e * 1024);
    } else {
#pragma unroll
      for (int e = 0; e < 8; ++e) glds16<NT_KV>(kbase, (unsigned)(((e >> 2) * 16 + c) * 256 + (e & 3) * 64 + g * 16), kv_l + e * 1024);
    }
  };

  const int k0 = ks * 1024 + wave * 256;
  const char* wo_base = (const char*)wo + (size_t)k0 * DM * 2 + nt * 256;
  const rsrc_t ro = make_rsrc(wo_base, 256 * DM * 2);
  const unsigned bo_off = (unsigned)(g * 32 * DM * 2 + c * 16);
  u32x4 so[4][8];
  const int rot = mt;

  {
    constexpr int DEPTH = 5, NSTEP = 8;
    u32x4 sb[DEPTH][8];
#pragma unroll
    for (int i = 0; i < 16; ++i) dma_x(rot >> 2, i);
    load_wq(sb[0], 0 ^ rot);
    load_wq(sb[1], 1 ^ rot);
    load_wq(sb[2], 2 ^ rot);
    load_wq(sb[3], 3 ^ rot);
    __builtin_amdgcn_sched_barrier(0);
    unsigned aoff[4];
#pragma unroll
    for (int s = 0; s < 4; ++s) aoff[s] = (unsigned)(c * 256 + (((4 * (s ^ (rot & 3)) + g) ^ c) << 4));
    auto read_a = [&](u32x4 (&at)[4], int s) {
#pragma unroll
      for (int m = 0; m < 4; ++m)
        at[m] = *(const u32x4*)(abuf + ((wave + m) & 3) * 4096 + ((s >> 2) ^ (rot >> 2)) * 16384 + aoff[s & 3]);
    };
    u32x4 at[2][4], bt[2][8];
    asm volatile("s_waitcnt vmcnt(32)" ::: "memory");
    read_a(at[0], 0);
    transpose8x8(sb[0], bt[0]);
    __builtin_amdgcn_sched_barrier(0);
    STAMP(0,1);
#pragma unroll
    for (int s = 0; s < NSTEP; ++s) {
      if (s < 3) {
#pragma unroll
        for (int i = 0; i < (s == 2 ? 6 : 5); ++i) dma_x(1 ^ (rot >> 2), s * 5 + i);
      }
      if (s + 4 < NSTEP) load_wq(sb[(s + 4) % DEPTH], (s + 4) ^ rot);
      if (s == 3) asm volatile("s_waitcnt vmcnt(16)" ::: "memory");
      if (s == 3) dma_kv();
      if (s + 1 < NSTEP) { read_a(at[(s + 1) & 1], s + 1); transpose8x8(sb[(s + 1) % DEPTH], bt[(s + 1) & 1]); }
      compute_stage(at[s & 1], bt[s & 1], acc);
      INTERLEAVE_STEP();
      __builtin_amdgcn_sched_barrier(0);
    }
  }
  STAMP(0,2);
  asm volatile("s_waitcnt vmcnt(0)" ::: "memory");
#define PF(a, n) do { __builtin_amdgcn_sched_barrier(0); _Pragma("unroll") for (int u_ = (a); u_ < (a) + (n); ++u_) { \
    const int ta_ = (u_ >> 3) ^ rot; \
    so[u_ >> 3][u_ & 7] = bload<NT_WO>(ro, bo_off, (unsigned)((128 * (ta_ >> 2) + 8 * (ta_ & 3) + (u_ & 7)) * DM * 2)); } \
    __builtin_amdgcn_sched_barrier(0); } while (0)
  {
    f32x4 q[8];
    PF(0, 3);
#pragma unroll
    for (int m = 1; m < 4; ++m) {
#pragma unroll
      for (int j = 0; j < 8; ++j) *(f32x4*)(lds + wave * 32768 + ((m - 1) * 8 + j) * 1024 + lane * 16) = acc[m][j];
      if (m == 1) PF(3, 2); else if (m == 2) PF(5, 2); else PF(7, 2);
    }
    __syncthreads();
#pragma unroll
    for (int j = 0; j < 8; ++j) q[j] = acc[0][j];
#pragma unroll
    for (int m = 1; m < 4; ++m) {
      const int w = (wave - m) & 3;
#pragma unroll
      for (int j = 0; j < 8; ++j) q[j] += *(const f32x4*)(lds + w * 32768 + ((m - 1) * 8 + j) * 1024 + lane * 16);
      if (m == 1) PF(9, 2); else if (m == 2) PF(11, 2); else PF(13, 2);
    }
    STAMP(0,3);
    char* qimg = abuf + 24 * 1024;
#pragma unroll
    for (int r = 0; r < 4; ++r) {
      u32x4 v;
#pragma unroll
      for (int p = 0; p < 4; ++p) v[p] = pack2(q[2 * p][r], q[2 * p + 1][r]);
      *(u32x4*)(qimg + (4 * g + r) * QROW + c * 16) = v;
    }
    __builtin_amdgcn_wave_barrier();
    PF(15, 3);
    u32x4 qf[4];
#pragma unroll
    for (int s = 0; s < 4; ++s) qf[s] = *(const u32x4*)(qimg + c * QROW + s * 64 + g * 16);
    u32x4 kf[2][4], vraw[8];
#pragma unroll
    for (int e = 0; e < 8; ++e) kf[e >> 2][e & 3] = *(const u32x4*)(kvbuf + e * 1024 + lane * 16);
#pragma unroll
    for (int e = 0; e < 8; ++e) vraw[e] = *(const u32x4*)(kvbuf + 8192 + e * 1024 + lane * 16);

    f32x4 sacc[2];
#pragma unroll
    for (int kt = 0; kt < 2; ++kt) {
      sacc[kt] = (f32x4){0.f, 0.f, 0.f, 0.f};
#pragma unroll
      for (int s = 0; s < 4; ++s) sacc[kt] = mfma16(kf[kt][s], qf[s], sacc[kt]);
    }
    PF(18, 3);
    const float scale = 0.08838834764831845f;
    float sv[8];
#pragma unroll
    for (int e = 0; e < 8; ++e) sv[e] = bf16_round(bf16_round(sacc[e >> 2][e & 3]) * scale);
    float mx = sv[0];
#pragma unroll
    for (int e = 1; e < 8; ++e) mx = fmaxf(mx, sv[e]);
    PF(21, 3);
    mx = fmaxf(mx, __shfl_xor(mx, 16));
    mx = fmaxf(mx, __shfl_xor(mx, 32));
    PF(24, 3);
    float sum = 0.f;
#pragma unroll
    for (int e = 0; e < 8; ++e) { sv[e] = __expf(sv[e] - mx); sum += sv[e]; }
    PF(27, 3);
    sum += __shfl_xor(sum, 16);
    sum += __shfl_xor(sum, 32);
    PF(30, 2);
    const float inv = 1.0f / sum;
    u32x4 pf;
#pragma unroll
    for (int p = 0; p < 4; ++p) pf[p] = pack2(sv[2 * p] * inv, sv[2 * p + 1] * inv);

    u32x4 vt[8];
    transpose8x8(vraw, vt);
    f32x4 oacc[8];
#pragma unroll
    for (int j = 0; j < 8; ++j) oacc[j] = mfma16(vt[j], pf, (f32x4){0.f, 0.f, 0.f, 0.f});

    const rsrc_t rO = make_rsrc(ws + WS_O, (unsigned)O_BYTES);
#pragma unroll
    for (int r = 0; r < 4; ++r) {
      u32x4 v;
#pragma unroll
      for (int p = 0; p < 4; ++p) v[p] = pack2(oacc[2 * p][r], oacc[2 * p + 1][r]);
      bstore_sc1(v, rO, (unsigned)(lane * 16), (unsigned)(((mt * 4 + wave) * 128 + h * 4 + r) * 1024));
    }
  }
  STAMP(0,4);
  asm volatile("s_waitcnt vmcnt(0)" ::: "memory");
  __syncthreads();
  if (wave == 0 && lane < 8) {
    const int cb = (mt * 4 + (lane & 3)) * 8 + (h >> 3) * 2 + (lane >> 2);
    __hip_atomic_store((u64*)(ws + WS_FLAG_O + cb * 128 + (h & 7) * 8), TAG, __ATOMIC_RELAXED, __HIP_MEMORY_SCOPE_AGENT);
  }
  STAMP(0,5);

#pragma unroll
  for (int t = 4; t < 8; ++t)
#pragma unroll
    for (int i = 0; i < 8; ++i)
      glds16<NT_WO>(wo_base, bo_off + (unsigned)((128 * ((t ^ rot) >> 2) + 8 * ((t ^ rot) & 3) + i) * DM * 2), abuf_l + ((t - 4) * 8 + i) * 1024);

  if (wave == 0) {
    u64* fl = (u64*)(ws + WS_FLAG_O + bid * 128) + (lane & 7);
    for (unsigned spin = 0; spin < SPIN_CAP; ++spin) {
      const u64 v = __hip_atomic_load(fl, __ATOMIC_RELAXED, __HIP_MEMORY_SCOPE_AGENT);
      if (__all(v == TAG)) break;
      __builtin_amdgcn_s_sleep(2);
    }
    if (lane < 8) __hip_atomic_store(fl, 0ull, __ATOMIC_RELAXED, __HIP_MEMORY_SCOPE_AGENT);
  }
  __syncthreads();
  __builtin_amdgcn_fence(__ATOMIC_ACQUIRE, "wavefront");
  STAMP(1,0);
#pragma unroll
  for (int m = 0; m < 4; ++m)
#pragma unroll
    for (int j = 0; j < 8; ++j) acc[m][j] = (f32x4){0.f, 0.f, 0.f, 0.f};
  {
    const rsrc_t rA = make_rsrc(ws + WS_O, (unsigned)O_BYTES);
    unsigned a_soff[4];
#pragma unroll
    for (int m = 0; m < 4; ++m) a_soff[m] = (unsigned)(((mt * 4 + ((wave + m) & 3)) * 128 + (k0 >> 5)) * 1024);
    constexpr int DA = 4, NSTEP = 8;
    u32x4 sa[DA][4];
    auto load_a = [&](int d, int t) {
#pragma unroll
      for (int m = 0; m < 4; ++m) sa[d][m] = bload_sc1(rA, (unsigned)(lane * 16), a_soff[m] + (t ^ rot) * 1024);
    };
#pragma unroll
    for (int t = 0; t < DA; ++t) load_a(t, t);
    u32x4 bt[2][8];
    transpose8x8(so[0], bt[0]);
    __builtin_amdgcn_sched_barrier(0);
#pragma unroll
    for (int t = 0; t < NSTEP; ++t) {
      u32x4 at[4];
#pragma unroll
      for (int m = 0; m < 4; ++m) at[m] = sa[t % DA][m];
      __builtin_amdgcn_sched_barrier(0);
      if (t == 0) STAMP(1,1);
      if (t + DA < NSTEP) load_a(t % DA, t + DA);
      if (t + 1 < 4) transpose8x8(so[t + 1], bt[(t + 1) & 1]);
      else if (t + 1 < NSTEP) {
        u32x4 raw[8];
#pragma unroll
        for (int i = 0; i < 8; ++i) raw[i] = *(const u32x4*)(abuf + ((t + 1 - 4) * 8 + i) * 1024 + lane * 16);
        transpose8x8(raw, bt[(t + 1) & 1]);
      }
      compute_stage(at, bt[t & 1], acc);
      INTERLEAVE_STEP();
      __builtin_amdgcn_sched_barrier(0);
    }
  }
  STAMP(1,2);
  f32x4 r[8];
  reduce_waves(lds, acc, r, wave, lane);
  STAMP(1,3);

  const int tile = mt * 8 + nt;
  const rsrc_t rs = make_rsrc(ws + WS_SLAB, (unsigned)SLAB_BYTES);
  const unsigned sl_off = (unsigned)((tile * 16 + wave) * 8192 + lane * 16);
#pragma unroll
  for (int j = 0; j < 8; ++j) bstore_sc1(__builtin_bit_cast(u32x4, r[j]), rs, sl_off, (unsigned)(ks * 32768 + j * 1024));
  asm volatile("s_waitcnt vmcnt(0)" ::: "memory");
  __syncthreads();
  if (wave == 0 && lane < 4) {
    const int ob = (mt * 4 + (nt & 3)) * 8 + lane * 2 + (nt >> 2);
    __hip_atomic_store((u64*)(ws + WS_FLAG_S + ob * 128 + ks * 8), TAG, __ATOMIC_RELAXED, __HIP_MEMORY_SCOPE_AGENT);
  }
  STAMP(1,4);
  if (wave != ks) { RSTAMP(1,7); return; }
  {
    u64* fl = (u64*)(ws + WS_FLAG_S + bid * 128) + (lane & 3);
    for (unsigned spin = 0; spin < SPIN_CAP; ++spin) {
      const u64 v = __hip_atomic_load(fl, __ATOMIC_RELAXED, __HIP_MEMORY_SCOPE_AGENT);
      if (__all(v == TAG)) break;
      __builtin_amdgcn_s_sleep(1);
    }
    if (lane < 4) __hip_atomic_store(fl, 0ull, __ATOMIC_RELAXED, __HIP_MEMORY_SCOPE_AGENT);
  }
  __builtin_amdgcn_fence(__ATOMIC_ACQUIRE, "wavefront");
  f32x4 s0[8], s1[8], s2[8], s3[8];
#pragma unroll
  for (int j = 0; j < 8; ++j) {
    s0[j] = __builtin_bit_cast(f32x4, bload_sc1(rs, sl_off, (unsigned)(0 * 32768 + j * 1024)));
    s1[j] = __builtin_bit_cast(f32x4, bload_sc1(rs, sl_off, (unsigned)(1 * 32768 + j * 1024)));
    s2[j] = __builtin_bit_cast(f32x4, bload_sc1(rs, sl_off, (unsigned)(2 * 32768 + j * 1024)));
    s3[j] = __builtin_bit_cast(f32x4, bload_sc1(rs, sl_off, (unsigned)(3 * 32768 + j * 1024)));
  }
#pragma unroll
  for (int j = 0; j < 8; ++j) r[j] = (s0[j] + s1[j]) + (s2[j] + s3[j]);
  char* obase = (char*)out + (size_t)(mt * 64 + wave * 16 + 4 * g) * DM * 2 + nt * 256 + c * 16;
#pragma unroll
  for (int rr = 0; rr < 4; ++rr) {
    u32x4 v;
#pragma unroll
    for (int p = 0; p < 4; ++p) v[p] = pack2(r[2 * p][rr], r[2 * p + 1][rr]);
    *(u32x4*)(obase + (size_t)rr * DM * 2) = v;
  }
  DRAIN_STAMP(); STAMP(1,5); RSTAMP(1,7);
}

extern "C" void kernel_launch(void* const* d_in, const int* in_sizes, int n_in,
                              void* d_out, int out_size, void* d_ws, size_t ws_size,
                              hipStream_t stream) {
  const __hip_bfloat16* x  = (const __hip_bfloat16*)d_in[0];
  const __hip_bfloat16* kc = (const __hip_bfloat16*)d_in[1];
  const __hip_bfloat16* vc = (const __hip_bfloat16*)d_in[2];
  const __hip_bfloat16* wq = (const __hip_bfloat16*)d_in[3];
  const __hip_bfloat16* wo = (const __hip_bfloat16*)d_in[4];
  __hip_bfloat16* out = (__hip_bfloat16*)d_out;
  char* ws = (char*)d_ws;
  (void)hipFuncSetAttribute((const void*)k_fused, hipFuncAttributeMaxDynamicSharedMemorySize, LDS_BYTES);
  k_fused<<<256, 256, LDS_BYTES, stream>>>(x, kc, vc, wq, wo, ws, out);
}
```

```cpp
#include <hip/hip_runtime.h>
#include <hip/hip_bf16.h>
#include <stdint.h>

typedef short bf16x8 __attribute__((ext_vector_type(8)));
typedef float f32x4 __attribute__((ext_vector_type(4)));
typedef unsigned u32x4 __attribute__((ext_vector_type(4)));
typedef unsigned long long u64;

#define DI __device__ __forceinline__

constexpr int NB = 16, SQ = 32, DM = 1024, NH = 32, SK = 32, DH = 128;
constexpr int MROWS = NB * SQ;
constexpr int HD = NH * DH;

constexpr size_t WS_FLAG_O = 0;
constexpr size_t WS_FLAG_S = 256 * 128;
constexpr size_t WS_O = 2 * 256 * 128;
constexpr size_t O_BYTES = (size_t)MROWS * HD * 2;
constexpr size_t WS_SLAB = WS_O + O_BYTES;
constexpr size_t SLAB_BYTES = (size_t)64 * 4 * 32768;
constexpr u64 TAG = 0x9E3779B97F4A7C15ull;
constexpr unsigned SPIN_CAP = 1u << 20;

DI f32x4 mfma16(u32x4 a, u32x4 b, f32x4 c) {
  return __builtin_amdgcn_mfma_f32_16x16x32_bf16(__builtin_bit_cast(bf16x8, a), __builtin_bit_cast(bf16x8, b), c, 0, 0, 0);
}
typedef __bf16 bf16x2_t __attribute__((ext_vector_type(2)));
typedef float f32x2 __attribute__((ext_vector_type(2)));
DI unsigned pack2(float lo, float hi) {
  f32x2 f = {lo, hi};
  return __builtin_bit_cast(unsigned, __builtin_convertvector(f, bf16x2_t));
}
DI float bf16_round(float f) { return __uint_as_float(pack2(f, 0.f) << 16); }

DI void transpose8x8(const u32x4 (&raw)[8], u32x4 (&out)[8]) {
#pragma unroll
  for (int d = 0; d < 4; ++d) {
#pragma unroll
    for (int p = 0; p < 4; ++p) {
      out[2 * d][p]     = __builtin_amdgcn_perm(raw[2 * p + 1][d], raw[2 * p][d], 0x05040100u);
      out[2 * d + 1][p] = __builtin_amdgcn_perm(raw[2 * p + 1][d], raw[2 * p][d], 0x07060302u);
    }
  }
}

constexpr int NT_WQ = 0, NT_X = 0, NT_KV = 0, NT_WO = 0;
template <int NT>
DI void glds16(const char* sbase, unsigned voff, unsigned lds_dst) {
  unsigned keep;
  if (NT)
    asm volatile("s_mov_b32 %0, m0\n\ts_mov_b32 m0, %3\n\ts_nop 0\n\tglobal_load_lds_dwordx4 %1, %2 nt\n\ts_mov_b32 m0, %0"
                 : "=&s"(keep) : "v"(voff), "s"(sbase), "s"(lds_dst) : "memory");
  else
    asm volatile("s_mov_b32 %0, m0\n\ts_mov_b32 m0, %3\n\ts_nop 0\n\tglobal_load_lds_dwordx4 %1, %2\n\ts_mov_b32 m0, %0"
                 : "=&s"(keep) : "v"(voff), "s"(sbase), "s"(lds_dst) : "memory");
}

typedef __amdgpu_buffer_rsrc_t rsrc_t;
DI rsrc_t make_rsrc(const void* p, unsigned bytes) { return __builtin_amdgcn_make_buffer_rsrc((void*)p, 0, (int)bytes, 0x00020000); }
template <int NT> DI u32x4 bload(rsrc_t rs, unsigned voff, unsigned soff) { return __builtin_amdgcn_raw_buffer_load_b128(rs, (int)voff, (int)soff, NT ? 2 : 0); }
DI u32x4 bload_sc1(rsrc_t rs, unsigned voff, unsigned soff) { return __builtin_amdgcn_raw_buffer_load_b128(rs, (int)voff, (int)soff, 16); }
DI void bstore_sc1(u32x4 v, rsrc_t rs, unsigned voff, unsigned soff) { __builtin_amdgcn_raw_buffer_store_b128(v, rs, (int)voff, (int)soff, 16); }

DI void compute_stage(const u32x4 (&sa)[4], const u32x4 (&bt)[8], f32x4 (&acc)[4][8]) {
#pragma unroll
  for (int j = 0; j < 8; ++j)
#pragma unroll
    for (int m = 0; m < 4; ++m) acc[m][j] = mfma16(sa[m], bt[j], acc[m][j]);
}

DI void reduce_waves(char* lds, const f32x4 (&acc)[4][8], f32x4 (&res)[8], int wave, int lane) {
#pragma unroll
  for (int m = 1; m < 4; ++m)
#pragma unroll
    for (int j = 0; j < 8; ++j) *(f32x4*)(lds + wave * 32768 + ((m - 1) * 8 + j) * 1024 + lane * 16) = acc[m][j];
  __syncthreads();
#pragma unroll
  for (int j = 0; j < 8; ++j) res[j] = acc[0][j];
#pragma unroll
  for (int m = 1; m < 4; ++m) {
    const int w = (wave - m) & 3;
#pragma unroll
    for (int j = 0; j < 8; ++j) res[j] += *(const f32x4*)(lds + w * 32768 + ((m - 1) * 8 + j) * 1024 + lane * 16);
  }
}

#define INTERLEAVE_STEP()                                                                   \
  _Pragma("unroll") for (int i_ = 0; i_ < 32; ++i_) {                                       \
    __builtin_amdgcn_sched_group_barrier(0x008, 1, 0);                                      \
    if (i_ < 24 && (i_ & 1)) __builtin_amdgcn_sched_group_barrier(0x020, 1, 0);             \
    if (i_ >= 8 && i_ < 20) __builtin_amdgcn_sched_group_barrier(0x100, 1, 0);              \
    if (i_ >= 16) __builtin_amdgcn_sched_group_barrier(0x002, 2, 0);                        \
  }

constexpr int QROW = 272;
constexpr int LDS_BYTES = 5 * 32768;

__global__ __launch_bounds__(256, 1) void k_fused(const __hip_bfloat16* __restrict__ x,
                                                  const __hip_bfloat16* __restrict__ kc,
                                                  const __hip_bfloat16* __restrict__ vc,
                                                  const __hip_bfloat16* __restrict__ wq,
                                                  const __hip_bfloat16* __restrict__ wo,
                                                  char* __restrict__ ws,
                                                  __hip_bfloat16* __restrict__ out) {
  extern __shared__ __attribute__((aligned(16))) char lds[];
  const int tid = threadIdx.x, lane = tid & 63;
  const int wave = __builtin_amdgcn_readfirstlane(tid >> 6);
  const int c = lane & 15, g = lane >> 4;
  const int bid = blockIdx.x;
  const int xg = bid & 7, idx = bid >> 3;
  const int mt = idx >> 2;
  const int h = xg + 8 * (idx & 3);
  const int ks = xg >> 1, nt = (xg & 1) * 4 + (idx & 3);


  char* abuf = lds + wave * 32768;
  const unsigned abuf_l = (unsigned)(uintptr_t)(__attribute__((address_space(3))) char*)abuf;
  f32x4 acc[4][8];
#pragma unroll
  for (int m = 0; m < 4; ++m)
#pragma unroll
    for (int j = 0; j < 8; ++j) acc[m][j] = (f32x4){0.f, 0.f, 0.f, 0.f};

  const char* xrow = (const char*)x + (size_t)(mt * 64) * (DM * 2) + wave * 512;
  auto dma_x = [&](int hh, int i) {
    const int row = 4 * i + g;
    const unsigned off = (unsigned)(row * (DM * 2) + hh * 256 + ((c ^ (row & 15)) << 4));
    glds16<NT_X>(xrow, off, abuf_l + hh * 16384 + i * 1024);
  };
  const rsrc_t rq = make_rsrc((const char*)wq + (size_t)(wave * 256) * HD * 2 + h * DH * 2, 256 * HD * 2);
  const unsigned bq_off = (unsigned)(g * 8 * HD * 2 + c * 16);
  auto load_wq = [&](u32x4 (&sb)[8], int step) {
#pragma unroll
    for (int i = 0; i < 8; ++i) sb[i] = bload<NT_WQ>(rq, bq_off, (unsigned)((step * 32 + i) * HD * 2));
  };
  const int b = mt * 2 + (wave >> 1);
  const char* kbase = (const char*)kc + (size_t)(b * NH + h) * SK * DH * 2;
  const char* vbase = (const char*)vc + (size_t)(b * NH + h) * SK * DH * 2;
  char* kvbuf = lds + 4 * 32768 + (wave >> 1) * 16384;
  auto dma_kv = [&]() {
    const unsigned kv_l = (unsigned)(uintptr_t)(__attribute__((address_space(3))) char*)kvbuf;
    if (wave & 1) {
#pragma unroll
      for (int e = 0; e < 8; ++e) glds16<NT_KV>(vbase, (unsigned)((16 * (e >> 2) + 4 * g + (e & 3)) * 256 + c * 16), kv_l + 8192 + e * 1024);
    } else {
#pragma unroll
      for (int e = 0; e < 8; ++e) glds16<NT_KV>(kbase, (unsigned)(((e >> 2) * 16 + c) * 256 + (e & 3) * 64 + g * 16), kv_l + e * 1024);
    }
  };

  const int k0 = ks * 1024 + wave * 256;
  const char* wo_base = (const char*)wo + (size_t)k0 * DM * 2 + nt * 256;
  const rsrc_t ro = make_rsrc(wo_base, 256 * DM * 2);
  const unsigned bo_off = (unsigned)(g * 32 * DM * 2 + c * 16);
  u32x4 so[4][8];
  const int rot = mt;

  {
    constexpr int DEPTH = 4, NSTEP = 8;
    u32x4 sb[DEPTH][8];
#pragma unroll
    for (int i = 0; i < 16; ++i) dma_x(rot >> 2, i);
    load_wq(sb[0], 0 ^ rot);
    load_wq(sb[1], 1 ^ rot);
    load_wq(sb[2], 2 ^ rot);
    __builtin_amdgcn_sched_barrier(0);
    unsigned aoff[4];
#pragma unroll
    for (int s = 0; s < 4; ++s) aoff[s] = (unsigned)(c * 256 + (((4 * (s ^ (rot & 3)) + g) ^ c) << 4));
    auto read_a = [&](u32x4 (&at)[4], int s) {
#pragma unroll
      for (int m = 0; m < 4; ++m)
        at[m] = *(const u32x4*)(abuf + ((wave + m) & 3) * 4096 + ((s >> 2) ^ (rot >> 2)) * 16384 + aoff[s & 3]);
    };
    u32x4 at[2][4], bt[2][8];
    asm volatile("s_waitcnt vmcnt(24)" ::: "memory");
    read_a(at[0], 0);
    transpose8x8(sb[0], bt[0]);
    __builtin_amdgcn_sched_barrier(0);

#pragma unroll
    for (int s = 0; s < NSTEP; ++s) {
      if (s < 3) {
#pragma unroll
        for (int i = 0; i < (s == 2 ? 6 : 5); ++i) dma_x(1 ^ (rot >> 2), s * 5 + i);
      }
      if (s + 3 < NSTEP) load_wq(sb[(s + 3) % DEPTH], (s + 3) ^ rot);
      if (s == 4) dma_kv();
      if (s == 3) asm volatile("s_waitcnt vmcnt(16)" ::: "memory");
      if (s + 1 < NSTEP) { read_a(at[(s + 1) & 1], s + 1); transpose8x8(sb[(s + 1) % DEPTH], bt[(s + 1) & 1]); }
      compute_stage(at[s & 1], bt[s & 1], acc);
      INTERLEAVE_STEP();
      __builtin_amdgcn_sched_barrier(0);
    }
  }

  asm volatile("s_waitcnt vmcnt(0)" ::: "memory");
#define PF(a, n) do { __builtin_amdgcn_sched_barrier(0); _Pragma("unroll") for (int u_ = (a); u_ < (a) + (n); ++u_) { \
    const int ta_ = (u_ >> 3) ^ rot; \
    so[u_ >> 3][u_ & 7] = bload<NT_WO>(ro, bo_off, (unsigned)((128 * (ta_ >> 2) + 8 * (ta_ & 3) + (u_ & 7)) * DM * 2)); } \
    __builtin_amdgcn_sched_barrier(0); } while (0)
  {
    f32x4 q[8];
    PF(0, 3);
#pragma unroll
    for (int m = 1; m < 4; ++m) {
#pragma unroll
      for (int j = 0; j < 8; ++j) *(f32x4*)(lds + wave * 32768 + ((m - 1) * 8 + j) * 1024 + lane * 16) = acc[m][j];
      if (m == 1) PF(3, 2); else if (m == 2) PF(5, 2); else PF(7, 2);
    }
    __syncthreads();
#pragma unroll
    for (int j = 0; j < 8; ++j) q[j] = acc[0][j];
#pragma unroll
    for (int m = 1; m < 4; ++m) {
      const int w = (wave - m) & 3;
#pragma unroll
      for (int j = 0; j < 8; ++j) q[j] += *(const f32x4*)(lds + w * 32768 + ((m - 1) * 8 + j) * 1024 + lane * 16);
      if (m == 1) PF(9, 2); else if (m == 2) PF(11, 2); else PF(13, 2);
    }

    char* qimg = abuf + 24 * 1024;
#pragma unroll
    for (int r = 0; r < 4; ++r) {
      u32x4 v;
#pragma unroll
      for (int p = 0; p < 4; ++p) v[p] = pack2(q[2 * p][r], q[2 * p + 1][r]);
      *(u32x4*)(qimg + (4 * g + r) * QROW + c * 16) = v;
    }
    __builtin_amdgcn_wave_barrier();
    PF(15, 3);
    u32x4 qf[4];
#pragma unroll
    for (int s = 0; s < 4; ++s) qf[s] = *(const u32x4*)(qimg + c * QROW + s * 64 + g * 16);
    u32x4 kf[2][4], vraw[8];
#pragma unroll
    for (int e = 0; e < 8; ++e) kf[e >> 2][e & 3] = *(const u32x4*)(kvbuf + e * 1024 + lane * 16);
#pragma unroll
    for (int e = 0; e < 8; ++e) vraw[e] = *(const u32x4*)(kvbuf + 8192 + e * 1024 + lane * 16);

    f32x4 sacc[2];
#pragma unroll
    for (int kt = 0; kt < 2; ++kt) {
      sacc[kt] = (f32x4){0.f, 0.f, 0.f, 0.f};
#pragma unroll
      for (int s = 0; s < 4; ++s) sacc[kt] = mfma16(kf[kt][s], qf[s], sacc[kt]);
    }
    PF(18, 3);
    const float scale = 0.08838834764831845f;
    float sv[8];
#pragma unroll
    for (int e = 0; e < 8; ++e) sv[e] = bf16_round(bf16_round(sacc[e >> 2][e & 3]) * scale);
    float mx = sv[0];
#pragma unroll
    for (int e = 1; e < 8; ++e) mx = fmaxf(mx, sv[e]);
    PF(21, 3);
    mx = fmaxf(mx, __shfl_xor(mx, 16));
    mx = fmaxf(mx, __shfl_xor(mx, 32));
    PF(24, 3);
    float sum = 0.f;
#pragma unroll
    for (int e = 0; e < 8; ++e) { sv[e] = __expf(sv[e] - mx); sum += sv[e]; }
    PF(27, 3);
    sum += __shfl_xor(sum, 16);
    sum += __shfl_xor(sum, 32);
    PF(30, 2);
    const float inv = 1.0f / sum;
    u32x4 pf;
#pragma unroll
    for (int p = 0; p < 4; ++p) pf[p] = pack2(sv[2 * p] * inv, sv[2 * p + 1] * inv);

    u32x4 vt[8];
    transpose8x8(vraw, vt);
    f32x4 oacc[8];
#pragma unroll
    for (int j = 0; j < 8; ++j) oacc[j] = mfma16(vt[j], pf, (f32x4){0.f, 0.f, 0.f, 0.f});

    const rsrc_t rO = make_rsrc(ws + WS_O, (unsigned)O_BYTES);
#pragma unroll
    for (int r = 0; r < 4; ++r) {
      u32x4 v;
#pragma unroll
      for (int p = 0; p < 4; ++p) v[p] = pack2(oacc[2 * p][r], oacc[2 * p + 1][r]);
      bstore_sc1(v, rO, (unsigned)(lane * 16), (unsigned)(((mt * 4 + wave) * 128 + h * 4 + r) * 1024));
    }
  }

  asm volatile("s_waitcnt vmcnt(0)" ::: "memory");
  __syncthreads();
  if (wave == 0 && lane < 8) {
    const int cb = (mt * 4 + (lane & 3)) * 8 + (h >> 3) * 2 + (lane >> 2);
    __hip_atomic_store((u64*)(ws + WS_FLAG_O + cb * 128 + (h & 7) * 8), TAG, __ATOMIC_RELAXED, __HIP_MEMORY_SCOPE_AGENT);
  }


#pragma unroll
  for (int t = 4; t < 8; ++t)
#pragma unroll
    for (int i = 0; i < 8; ++i)
      glds16<NT_WO>(wo_base, bo_off + (unsigned)((128 * ((t ^ rot) >> 2) + 8 * ((t ^ rot) & 3) + i) * DM * 2), abuf_l + ((t - 4) * 8 + i) * 1024);

  if (wave == 0) {
    u64* fl = (u64*)(ws + WS_FLAG_O + bid * 128) + (lane & 7);
    for (unsigned spin = 0; spin < SPIN_CAP; ++spin) {
      const u64 v = __hip_atomic_load(fl, __ATOMIC_RELAXED, __HIP_MEMORY_SCOPE_AGENT);
      if (__all(v == TAG)) break;
      __builtin_amdgcn_s_sleep(2);
    }
    if (lane < 8) __hip_atomic_store(fl, 0ull, __ATOMIC_RELAXED, __HIP_MEMORY_SCOPE_AGENT);
  }
  __syncthreads();
  __builtin_amdgcn_fence(__ATOMIC_ACQUIRE, "wavefront");

#pragma unroll
  for (int m = 0; m < 4; ++m)
#pragma unroll
    for (int j = 0; j < 8; ++j) acc[m][j] = (f32x4){0.f, 0.f, 0.f, 0.f};
  {
    const rsrc_t rA = make_rsrc(ws + WS_O, (unsigned)O_BYTES);
    unsigned a_soff[4];
#pragma unroll
    for (int m = 0; m < 4; ++m) a_soff[m] = (unsigned)(((mt * 4 + ((wave + m) & 3)) * 128 + (k0 >> 5)) * 1024);
    constexpr int DA = 4, NSTEP = 8;
    u32x4 sa[DA][4];
    auto load_a = [&](int d, int t) {
#pragma unroll
      for (int m = 0; m < 4; ++m) sa[d][m] = bload_sc1(rA, (unsigned)(lane * 16), a_soff[m] + (t ^ rot) * 1024);
    };
#pragma unroll
    for (int t = 0; t < DA; ++t) load_a(t, t);
    u32x4 bt[2][8];
    transpose8x8(so[0], bt[0]);
    __builtin_amdgcn_sched_barrier(0);
#pragma unroll
    for (int t = 0; t < NSTEP; ++t) {
      u32x4 at[4];
#pragma unroll
      for (int m = 0; m < 4; ++m) at[m] = sa[t % DA][m];
      __builtin_amdgcn_sched_barrier(0);
      if (t + DA < NSTEP) load_a(t % DA, t + DA);
      if (t + 1 < 4) transpose8x8(so[t + 1], bt[(t + 1) & 1]);
      else if (t + 1 < NSTEP) {
        u32x4 raw[8];
#pragma unroll
        for (int i = 0; i < 8; ++i) raw[i] = *(const u32x4*)(abuf + ((t + 1 - 4) * 8 + i) * 1024 + lane * 16);
        transpose8x8(raw, bt[(t + 1) & 1]);
      }
      compute_stage(at, bt[t & 1], acc);
      INTERLEAVE_STEP();
      __builtin_amdgcn_sched_barrier(0);
    }
  }

  f32x4 r[8];
  reduce_waves(lds, acc, r, wave, lane);


  const int tile = mt * 8 + nt;
  const rsrc_t rs = make_rsrc(ws + WS_SLAB, (unsigned)SLAB_BYTES);
  const unsigned sl_off = (unsigned)((tile * 16 + wave) * 8192 + lane * 16);
#pragma unroll
  for (int j = 0; j < 8; ++j) bstore_sc1(__builtin_bit_cast(u32x4, r[j]), rs, sl_off, (unsigned)(ks * 32768 + j * 1024));
  asm volatile("s_waitcnt vmcnt(0)" ::: "memory");
  __syncthreads();
  if (wave == 0 && lane < 4) {
    const int ob = (mt * 4 + (nt & 3)) * 8 + lane * 2 + (nt >> 2);
    __hip_atomic_store((u64*)(ws + WS_FLAG_S + ob * 128 + ks * 8), TAG, __ATOMIC_RELAXED, __HIP_MEMORY_SCOPE_AGENT);
  }

  if (wave != ks) return;
  {
    u64* fl = (u64*)(ws + WS_FLAG_S + bid * 128) + (lane & 3);
    for (unsigned spin = 0; spin < SPIN_CAP; ++spin) {
      const u64 v = __hip_atomic_load(fl, __ATOMIC_RELAXED, __HIP_MEMORY_SCOPE_AGENT);
      if (__all(v == TAG)) break;
      __builtin_amdgcn_s_sleep(1);
    }
    if (lane < 4) __hip_atomic_store(fl, 0ull, __ATOMIC_RELAXED, __HIP_MEMORY_SCOPE_AGENT);
  }
  __builtin_amdgcn_fence(__ATOMIC_ACQUIRE, "wavefront");
  f32x4 s0[8], s1[8], s2[8], s3[8];
#pragma unroll
  for (int j = 0; j < 8; ++j) {
    s0[j] = __builtin_bit_cast(f32x4, bload_sc1(rs, sl_off, (unsigned)(0 * 32768 + j * 1024)));
    s1[j] = __builtin_bit_cast(f32x4, bload_sc1(rs, sl_off, (unsigned)(1 * 32768 + j * 1024)));
    s2[j] = __builtin_bit_cast(f32x4, bload_sc1(rs, sl_off, (unsigned)(2 * 32768 + j * 1024)));
    s3[j] = __builtin_bit_cast(f32x4, bload_sc1(rs, sl_off, (unsigned)(3 * 32768 + j * 1024)));
  }
#pragma unroll
  for (int j = 0; j < 8; ++j) r[j] = (s0[j] + s1[j]) + (s2[j] + s3[j]);
  char* obase = (char*)out + (size_t)(mt * 64 + wave * 16 + 4 * g) * DM * 2 + nt * 256 + c * 16;
#pragma unroll
  for (int rr = 0; rr < 4; ++rr) {
    u32x4 v;
#pragma unroll
    for (int p = 0; p < 4; ++p) v[p] = pack2(r[2 * p][rr], r[2 * p + 1][rr]);
    *(u32x4*)(obase + (size_t)rr * DM * 2) = v;
  }
}

extern "C" void kernel_launch(void* const* d_in, const int* in_sizes, int n_in,
                              void* d_out, int out_size, void* d_ws, size_t ws_size,
                              hipStream_t stream) {
  const __hip_bfloat16* x  = (const __hip_bfloat16*)d_in[0];
  const __hip_bfloat16* kc = (const __hip_bfloat16*)d_in[1];
  const __hip_bfloat16* vc = (const __hip_bfloat16*)d_in[2];
  const __hip_bfloat16* wq = (const __hip_bfloat16*)d_in[3];
  const __hip_bfloat16* wo = (const __hip_bfloat16*)d_in[4];
  __hip_bfloat16* out = (__hip_bfloat16*)d_out;
  char* ws = (char*)d_ws;
  (void)hipFuncSetAttribute((const void*)k_fused, hipFuncAttributeMaxDynamicSharedMemorySize, LDS_BYTES);
  k_fused<<<256, 256, LDS_BYTES, stream>>>(x, kc, vc, wq, wo, ws, out);
}
```
